```python
import torch

def _softmax(x, axis=-1):
    return torch.softmax(x, dim=axis)
def _gelu(x):
    return torch.nn.functional.gelu(x, approximate="tanh")
def _silu(x):
    return torch.nn.functional.silu(x)
def _sigmoid(x):
    return torch.sigmoid(x)
def _layernorm(x, axis=-1, eps=1e-5):
    m = x.mean(dim=axis, keepdim=True)
    v = x.var(dim=axis, keepdim=True, unbiased=False)
    return (x - m) / torch.sqrt(v + eps)
def _scatter_add(tgt, idx, src, axis=0):
    return tgt.clone().index_add_(axis, idx, src)
def _segment_sum(src, seg, n):
    out = torch.zeros((n,) + src.shape[1:], dtype=src.dtype)
    return out.index_add_(0, seg, src)
def _pad(x, pw):
    flat = tuple(v for pair in reversed(pw) for v in pair)
    return torch.nn.functional.pad(x, flat)

kernel_name = 'sequential_preLN__ssm_conv_h3__glu_gated_geglu__prompt'

def setup_inputs():

    _w = (lambda a: (lambda t: t.to(torch.bfloat16) if t.is_floating_point() else t)(torch.as_tensor(a)))
    def _A_setup():
        B, S, D = 1, 2048, 1024
        ks = 4
        return {
            "q": _w((lambda *s: torch.randn(*s, dtype=torch.bfloat16) * 0.05)(B, S, D)),
            "k": _w((lambda *s: torch.randn(*s, dtype=torch.bfloat16) * 0.2)(B, S, D)),
            "v": _w((lambda *s: torch.randn(*s, dtype=torch.bfloat16) * 0.2)(B, S, D)),
            "shift_k": _w((lambda *s: torch.randn(*s, dtype=torch.bfloat16) * 0.2)(D, ks) * 0.5),
            "ssm_k": _w((lambda *s: torch.randn(*s, dtype=torch.bfloat16) * 0.4)(D, S) * (1.0 / S ** 0.5)),
            "ssm_D": _w((lambda *s: torch.randn(*s, dtype=torch.bfloat16) * 0.2)(D)),
        }
    _A_in = _A_setup()

    def _B_setup():
        B, S, Dm, Df = 1, 2048, 1024, 3584
        return {
            "x": _w((lambda *s: torch.randn(*s, dtype=torch.bfloat16) * 0.05)(B, S, Dm)),
            "w_gate": _w((lambda *s: torch.randn(*s, dtype=torch.bfloat16) * 0.4)(Dm, Df) * (Dm ** -0.5)),
            "w_up": _w((lambda *s: torch.randn(*s, dtype=torch.bfloat16) * 0.4)(Dm, Df) * (Dm ** -0.5)),
            "w_down": _w((lambda *s: torch.randn(*s, dtype=torch.bfloat16) * 0.4)(Df, Dm) * (Df ** -0.5)),
        }
    _B_in = _B_setup()

    return {
        "A_q": _A_in["q"],
        "A_k": _A_in["k"],
        "A_v": _A_in["v"],
        "A_shift_k": _A_in["shift_k"],
        "A_ssm_k": _A_in["ssm_k"],
        "A_ssm_D": _A_in["ssm_D"],
        "B_x": _B_in["x"],
        "B_w_gate": _B_in["w_gate"],
        "B_w_up": _B_in["w_up"],
        "B_w_down": _B_in["w_down"],
    }

def reference(A_q, A_k, A_v, A_shift_k, A_ssm_k, A_ssm_D, B_x, B_w_gate, B_w_up, B_w_down):
    def _A_ref(q, k, v, shift_k, ssm_k, ssm_D):
        B, S, D = q.shape
        ks = shift_k.shape[-1]
        k_pad = _pad(k, ((0, 0), (ks - 1, 0), (0, 0)))
        k_shift = torch.zeros_like(k)
        for t in range(ks):
            k_shift = k_shift + k_pad[:, t:t + S, :] * shift_k[:, ks - 1 - t][None, None, :]
        kv = k_shift * v
        L = 2 * S
        kv_pad = _pad(kv, ((0, 0), (0, S), (0, 0)))
        kf_pad = _pad(ssm_k, ((0, 0), (0, S)))
        kv_f = (lambda x, axes=(-2,-1): torch.fft.rfft2(x.float(), dim=axes))(kv_pad[:, None, :, :], axes=(1, 2))
        kf_f = (lambda x, axes=(-2,-1): torch.fft.rfft2(x.float(), dim=axes))(kf_pad[:, None, :], axes=(1, 2))
        kf_f = (lambda x, *d: x.permute(*d))(kf_f, 1, 2, 0)
        y_f = kv_f * kf_f[None, :, :, :]
        y = (lambda x, s, axes=(-2,-1): torch.fft.irfft2(x, s=s, dim=axes).to(torch.bfloat16))(y_f, (1, L), axes=(1, 2))[:, 0, :S, :]
        y = y + kv * ssm_D[None, None, :]
        return q * y

    def _B_ref(x, w_gate, w_up, w_down):
        g = torch.einsum("bsd,df->bsf", x, w_gate)
        u = torch.einsum("bsd,df->bsf", x, w_up)
        h = _gelu(g) * u
        return torch.einsum("bsf,fd->bsd", h, w_down)

    x = A_q
    h = x + _A_ref(_layernorm(x, axis=-1), A_k, A_v, A_shift_k, A_ssm_k, A_ssm_D)
    h2 = h + _B_ref(_layernorm(h, axis=-1), B_w_gate, B_w_up, B_w_down)
    return h2
```

```cpp
#include <hip/hip_runtime.h>
#include <hip/hip_bf16.h>
#include <stdint.h>

typedef unsigned short u16;
constexpr int S_ = 2048, D_ = 1024, F_ = 3584;

__device__ __forceinline__ float bf2f(u16 b) { return __uint_as_float(((unsigned)b) << 16); }
__device__ __forceinline__ u16 f2bf(float f) { unsigned u = __float_as_uint(f); u += 0x7FFFu + ((u >> 16) & 1u); return (u16)(u >> 16); }
__device__ __forceinline__ float rb(float f) { return bf2f(f2bf(f)); }

__global__ void k_ln(const u16* x, u16* out) {
  int row = blockIdx.x; int t = threadIdx.x;
  __shared__ float red[256];
  float v[4]; float s = 0;
  for (int i = 0; i < 4; i++) { v[i] = bf2f(x[row * D_ + t * 4 + i]); s += v[i]; }
  red[t] = s; __syncthreads();
  for (int o = 128; o > 0; o >>= 1) { if (t < o) red[t] += red[t + o]; __syncthreads(); }
  float mean = red[0] / D_; __syncthreads();
  float q = 0; for (int i = 0; i < 4; i++) { float d = v[i] - mean; q += d * d; }
  red[t] = q; __syncthreads();
  for (int o = 128; o > 0; o >>= 1) { if (t < o) red[t] += red[t + o]; __syncthreads(); }
  float var = red[0] / D_;
  float mb = rb(mean), vb = rb(var);
  float sd = rb(sqrtf(rb(vb + 1e-5f)));
  for (int i = 0; i < 4; i++) out[row * D_ + t * 4 + i] = f2bf(rb(v[i] - mb) / sd);
}

__global__ void k_kv(const u16* k, const u16* v, const u16* shk, u16* kv) {
  int idx = blockIdx.x * blockDim.x + threadIdx.x; int s = idx / D_, d = idx % D_;
  float acc = 0.f;
  for (int t = 0; t < 4; t++) { int ss = s + t - 3; float kk = ss >= 0 ? bf2f(k[ss * D_ + d]) : 0.f; acc = rb(acc + rb(kk * bf2f(shk[d * 4 + 3 - t]))); }
  kv[idx] = f2bf(acc * bf2f(v[idx]));
}

__global__ void k_conv(const u16* kv, const u16* ssmk, const u16* ssmD, const u16* qln, const u16* x, u16* h) {
  int idx = blockIdx.x * blockDim.x + threadIdx.x; int t = idx / D_, d = idx % D_;
  float acc = 0.f;
  for (int s = 0; s <= t; s++) acc += bf2f(kv[s * D_ + d]) * bf2f(ssmk[d * S_ + (t - s)]);
  float y = rb(acc);
  y = rb(y + rb(bf2f(kv[idx]) * bf2f(ssmD[d])));
  float a = rb(bf2f(qln[idx]) * y);
  h[idx] = f2bf(bf2f(x[idx]) + a);
}

template <int MODE>
__global__ void k_gemm(const u16* A, const u16* B, const u16* B2, const u16* R, u16* C, int M, int N, int K) {
  __shared__ float As[16][65], Bs[16][65], B2s[16][65];
  int tx = threadIdx.x % 16, ty = threadIdx.x / 16; int bm = blockIdx.y * 64, bn = blockIdx.x * 64;
  float acc[4][4] = {}, acc2[4][4] = {};
  for (int k0 = 0; k0 < K; k0 += 16) {
    for (int i = threadIdx.x; i < 16 * 64; i += 256) { int kk = i % 16, m = i / 16; As[kk][m] = bf2f(A[(size_t)(bm + m) * K + k0 + kk]); }
    for (int i = threadIdx.x; i < 16 * 64; i += 256) { int n = i % 64, kk = i / 64; Bs[kk][n] = bf2f(B[(size_t)(k0 + kk) * N + bn + n]); if (MODE == 0) B2s[kk][n] = bf2f(B2[(size_t)(k0 + kk) * N + bn + n]); }
    __syncthreads();
    for (int kk = 0; kk < 16; kk++)
      for (int i = 0; i < 4; i++) for (int j = 0; j < 4; j++) { acc[i][j] += As[kk][ty * 4 + i] * Bs[kk][tx * 4 + j]; if (MODE == 0) acc2[i][j] += As[kk][ty * 4 + i] * B2s[kk][tx * 4 + j]; }
    __syncthreads();
  }
  for (int i = 0; i < 4; i++) for (int j = 0; j < 4; j++) {
    size_t o = (size_t)(bm + ty * 4 + i) * N + bn + tx * 4 + j;
    if (MODE == 0) {
      float g = rb(acc[i][j]), u = rb(acc2[i][j]);
      float ge = rb(0.5f * g * (1.f + tanhf(0.7978845608028654f * (g + 0.044715f * g * g * g))));
      C[o] = f2bf(ge * u);
    } else {
      C[o] = f2bf(bf2f(R[o]) + rb(acc[i][j]));
    }
  }
}

extern "C" void kernel_launch(void* const* d_in, const int* in_sizes, int n_in, void* d_out, int out_size, void* d_ws, size_t ws_size, hipStream_t stream) {
  const u16* q = (const u16*)d_in[0]; const u16* k = (const u16*)d_in[1]; const u16* v = (const u16*)d_in[2];
  const u16* shk = (const u16*)d_in[3]; const u16* ssmk = (const u16*)d_in[4]; const u16* ssmD = (const u16*)d_in[5];
  const u16* wg = (const u16*)d_in[7]; const u16* wu = (const u16*)d_in[8]; const u16* wd = (const u16*)d_in[9];
  u16* ws = (u16*)d_ws; size_t SD = (size_t)S_ * D_;
  u16* qln = ws; u16* kv = ws + SD; u16* h = ws + 2 * SD; u16* hn = ws + 3 * SD; u16* H = ws + 4 * SD;
  k_ln<<<S_, 256, 0, stream>>>(q, qln);
  k_kv<<<SD / 256, 256, 0, stream>>>(k, v, shk, kv);
  k_conv<<<SD / 256, 256, 0, stream>>>(kv, ssmk, ssmD, qln, q, h);
  k_ln<<<S_, 256, 0, stream>>>(h, hn);
  k_gemm<0><<<dim3(F_ / 64, S_ / 64), 256, 0, stream>>>(hn, wg, wu, nullptr, H, S_, F_, D_);
  k_gemm<1><<<dim3(D_ / 64, S_ / 64), 256, 0, stream>>>(H, wd, nullptr, h, (u16*)d_out, S_, D_, F_);
}
```

```cpp
#include <hip/hip_runtime.h>
#include <stdint.h>

typedef unsigned short u16;
typedef unsigned u32;
typedef u32 u32x2 __attribute__((ext_vector_type(2)));
typedef u32 u32x4 __attribute__((ext_vector_type(4)));
typedef float f32x2 __attribute__((ext_vector_type(2)));
typedef __bf16 bf16x2 __attribute__((ext_vector_type(2)));
typedef short s16x4 __attribute__((ext_vector_type(4)));
#define LAS __attribute__((address_space(3)))
#define DI __device__ __forceinline__

constexpr int S_ = 2048, D_ = 1024, F_ = 3584;

DI float lo16(u32 w) { return __uint_as_float(w << 16); }
DI float hi16(u32 w) { return __uint_as_float(w & 0xffff0000u); }
DI u32 pk2(float lo, float hi) { f32x2 v = {lo, hi}; bf16x2 b = __builtin_convertvector(v, bf16x2); return __builtin_bit_cast(u32, b); }
DI float rb(float f) { return lo16(pk2(f, 0.f)); }
DI void st16_dev(void* p, u32x4 v) { asm volatile("global_store_dwordx4 %0, %1, off sc1\n\ts_nop 1" :: "v"(p), "v"(v) : "memory"); }
DI __amdgpu_buffer_rsrc_t mk_rsrc(const void* p, u32 bytes) {
  const size_t a = (size_t)p;
  const u32 lo = __builtin_amdgcn_readfirstlane((u32)a), hi = __builtin_amdgcn_readfirstlane((u32)(a >> 32));
  return __builtin_amdgcn_make_buffer_rsrc((void*)(((size_t)hi << 32) | lo), 0, __builtin_amdgcn_readfirstlane(bytes), 0x00020000);
}

DI void prefetch_256_lines(const void* p, u32 first, u32 nlines, int lane, u32& d, u32 cnt = 256) {
  u32 o[4];
#pragma unroll
  for (int i = 0; i < 4; ++i) { u32 r = 64 * i + lane; r = r < cnt ? r : cnt - 1; u32 l = first + r; l = l < nlines ? l : nlines - 1; o[i] = l * 128; }
  asm volatile("global_load_dword %0, %1, %5\n\tglobal_load_dword %0, %2, %5\n\tglobal_load_dword %0, %3, %5\n\tglobal_load_dword %0, %4, %5"
               : "+&v"(d) : "v"(o[0]), "v"(o[1]), "v"(o[2]), "v"(o[3]), "s"(p) : "memory");
}
typedef unsigned long long u64;
DI void signal_flag(u64* f, u64 tag) { __hip_atomic_store(f, tag, __ATOMIC_RELAXED, __HIP_MEMORY_SCOPE_AGENT); }
DI void poll_flags(const u64* f, int n, u64 tag, int lane) {
  const u64* a = f + (lane < n ? lane : 0);
  for (int spin = 0; spin < (1 << 16); ++spin) {
    const u64 v = __hip_atomic_load(a, __ATOMIC_RELAXED, __HIP_MEMORY_SCOPE_AGENT);
    if (__builtin_amdgcn_ballot_w64(v != tag) == 0) break;
    __builtin_amdgcn_s_sleep(2);
  }
}
DI void poll_flags256(const u64* f, u64 tag, int lane) {
  const auto srd = mk_rsrc(f, 2048);
  const u32 lo = (u32)tag, hi = (u32)(tag >> 32);
  for (int spin = 0; spin < (1 << 16); ++spin) {
    const u32x4 a = __builtin_amdgcn_raw_buffer_load_b128(srd, lane * 32, 0, 16  ), b = __builtin_amdgcn_raw_buffer_load_b128(srd, lane * 32 + 16, 0, 16  );
    const u32 bad = (a[0] ^ lo) | (a[1] ^ hi) | (a[2] ^ lo) | (a[3] ^ hi) | (b[0] ^ lo) | (b[1] ^ hi) | (b[2] ^ lo) | (b[3] ^ hi);
    if (__builtin_amdgcn_ballot_w64(bad != 0) == 0) break;
    __builtin_amdgcn_s_sleep(2);
  }
}
#define DRAIN() asm volatile("s_waitcnt vmcnt(0)" ::: "memory")
#define BARRIER() do { asm volatile("" ::: "memory"); __builtin_amdgcn_s_barrier(); asm volatile("" ::: "memory"); } while (0)

template <int CTRL> DI float dpp_add(float v) { return v + __builtin_bit_cast(float, __builtin_amdgcn_update_dpp(0, __builtin_bit_cast(int, v), CTRL, 0xf, 0xf, true)); }
DI float wave_sum(float v) {
  v = dpp_add<0xB1>(v);
  v = dpp_add<0x4E>(v);
  v = dpp_add<0x141>(v);
  v = dpp_add<0x140>(v);
  { auto r = __builtin_amdgcn_permlane16_swap(__float_as_uint(v), __float_as_uint(v), false, false); v = __uint_as_float(r[0]) + __uint_as_float(r[1]); }
  { auto r = __builtin_amdgcn_permlane32_swap(__float_as_uint(v), __float_as_uint(v), false, false); v = __uint_as_float(r[0]) + __uint_as_float(r[1]); }
  return v;
}
typedef float f32x32 __attribute__((ext_vector_type(32)));
typedef u32 u32x8 __attribute__((ext_vector_type(8)));
constexpr int KVP = S_ + 128, YBS = 16384 + 256;
constexpr int CSTR = 4160, XOFF = 2 * CSTR, WSTR = 13120;
constexpr int G1_WBASE = 65536, G1_UOFF = 14464, G1_WSTAGE = 28928;
constexpr int G2_STAGE = 32768, G2_WOFF = 16384;

extern "C" __global__ __launch_bounds__(256, 1) void km_main(const u16* __restrict__ kin, const u16* __restrict__ vin, const u16* __restrict__ shk, u16* __restrict__ kvr, const u16* __restrict__ ssmk, const u16* __restrict__ ssmD, u16* __restrict__ yb,
    const u16* __restrict__ x, u16* __restrict__ h, u16* __restrict__ hn, const u16* __restrict__ wg, const u16* __restrict__ wu, const u16* __restrict__ wd,
    u16* __restrict__ Hb, u32* __restrict__ xbuf, u64* __restrict__ fl, u16* __restrict__ out) {
  __shared__ __attribute__((aligned(1024))) char smem[4 * G2_STAGE > G1_WBASE + 3 * G1_WSTAGE ? 4 * G2_STAGE : G1_WBASE + 3 * G1_WSTAGE];
  const int tid = threadIdx.x, wid = __builtin_amdgcn_readfirstlane(tid >> 6), lane = tid & 63;
  const int bid = blockIdx.x, xcd = bid & 7, idx = bid >> 3;
  const u32 sbase = (u32)(size_t)(LAS char*)smem;
  u64* flag0 = fl + 16; u64* flagB = flag0 + 256; u64* flagC = flagB + 256; u64* flagA = flagC + 256;
  u32x2 epoch;
  u64 tag;
  const int pn = xcd * 4 + (idx & 3), pm = idx >> 2, tok0 = pm * 256, f0 = pn * 112;

  u32x4 xv[2][2];
  const int ch = bid * 4 + wid;
  LAS char* wb = (LAS char*)smem + wid * WSTR;
  float dd;
  {
    const int tg = lane >> 3, cgp = lane & 7, bt = bid & 15, bc = bid >> 4;
    const int t0 = bt * 128 + wid * 32 + tg * 4, c0 = bc * 64 + cgp * 8;
    u32x4 kk[7], vv[4], sh[4];
    {
      const auto srdKi = mk_rsrc(kin, S_ * D_ * 2), srdV = mk_rsrc(vin, S_ * D_ * 2), srdSh = mk_rsrc(shk, D_ * 4 * 2);
      const int ko = (t0 - 3) * 2048 + c0 * 2, vo2 = t0 * 2048 + c0 * 2;
      asm volatile(
          "s_nop 4\n\t"
          "buffer_load_dwordx4 %0, %15, %25, 0 offen nt\n\tbuffer_load_dwordx4 %1, %16, %25, 0 offen nt\n\tbuffer_load_dwordx4 %2, %17, %25, 0 offen nt\n\t"
          "buffer_load_dwordx4 %3, %18, %25, 0 offen nt\n\tbuffer_load_dwordx4 %4, %19, %25, 0 offen nt\n\tbuffer_load_dwordx4 %5, %20, %25, 0 offen nt\n\tbuffer_load_dwordx4 %6, %21, %25, 0 offen nt\n\t"
          "buffer_load_dwordx4 %7, %22, %26, 0 offen nt\n\tbuffer_load_dwordx4 %8, %22, %26, 0 offen offset:2048 nt\n\t"
          "buffer_load_dwordx4 %9, %23, %26, 0 offen nt\n\tbuffer_load_dwordx4 %10, %23, %26, 0 offen offset:2048 nt\n\t"
          "buffer_load_dwordx4 %11, %24, %27, 0 offen\n\tbuffer_load_dwordx4 %12, %24, %27, 0 offen offset:16\n\t"
          "buffer_load_dwordx4 %13, %24, %27, 0 offen offset:32\n\tbuffer_load_dwordx4 %14, %24, %27, 0 offen offset:48"
          : "=&v"(kk[0]), "=&v"(kk[1]), "=&v"(kk[2]), "=&v"(kk[3]), "=&v"(kk[4]), "=&v"(kk[5]), "=&v"(kk[6]), "=&v"(vv[0]), "=&v"(vv[1]), "=&v"(vv[2]), "=&v"(vv[3]),
            "=&v"(sh[0]), "=&v"(sh[1]), "=&v"(sh[2]), "=&v"(sh[3])
          : "v"(ko), "v"(ko + 2048), "v"(ko + 4096), "v"(ko + 6144), "v"(ko + 8192), "v"(ko + 10240), "v"(ko + 12288), "v"(vo2), "v"(vo2 + 4096), "v"(c0 * 8),
            "s"(srdKi), "s"(srdV), "s"(srdSh)
          : "memory");
    }
    asm volatile("global_load_dwordx2 %0, %1, %2 sc1" : "=v"(epoch) : "v"(0), "s"(fl) : "memory");
    u32 cpf = 0;
    {
      size_t c0a, c1a;
      asm volatile("s_getpc_b64 s[60:61]\n\ts_add_u32 s60, s60, km_main@rel32@lo+4\n\ts_addc_u32 s61, s61, km_main@rel32@hi+12\n\ts_mov_b64 %0, s[60:61]\n\t"
                   "s_getpc_b64 s[60:61]\n\ts_add_u32 s60, s60, k9_end@rel32@lo+4\n\ts_addc_u32 s61, s61, k9_end@rel32@hi+12\n\ts_mov_b64 %1, s[60:61]"
                   : "=s"(c0a), "=s"(c1a) :: "s60", "s61", "scc");
      const u32 nl = (u32)((c1a - c0a) >> 7) + 1;
      u32 l = idx * 20 + (lane < 5 ? lane : 4) + wid * 5;
      l = l < nl ? l : nl - 1;
      asm volatile("global_load_dword %0, %1, %2" : "+v"(cpf) : "v"(l * 128), "s"(c0a) : "memory");
    }
    u32 k0, dw;
    asm volatile("s_load_dword %0, %2, 0x0\n\ts_load_dword %1, %3, 0x0\n\ts_waitcnt lgkmcnt(0)" : "=&s"(k0), "=&s"(dw) : "s"(ssmk + (size_t)ch * S_), "s"((const u32*)ssmD + (ch >> 1)) : "memory");
    dd = (ch & 1) ? hi16(dw) : lo16(dw);
    if (lane < 32) *(LAS u32x4*)(wb + XOFF + 4096 + 16 * lane) = u32x4{0, 0, 0, 0};
    if (lane < 4) *(LAS u32x4*)(wb + (lane & 1) * CSTR + 16 * (lane >> 1)) = u32x4{0, 0, 0, lane == 3 ? k0 << 16 : 0u};
    asm volatile("s_waitcnt vmcnt(2)"
                 : "+v"(kk[0]), "+v"(kk[1]), "+v"(kk[2]), "+v"(kk[3]), "+v"(kk[4]), "+v"(kk[5]), "+v"(kk[6]), "+v"(vv[0]), "+v"(vv[1]), "+v"(vv[2]), "+v"(vv[3]),
                   "+v"(sh[0]), "+v"(sh[1]), "+v"(sh[2]), "+v"(sh[3]) :: "memory");
    u32x2 o[8];
#pragma unroll
    for (int j = 0; j < 8; ++j) {
      const u32 s01 = sh[j >> 1][(j & 1) * 2], s23 = sh[j >> 1][(j & 1) * 2 + 1];
      const float s0 = lo16(s01), s1 = hi16(s01), s2 = lo16(s23), s3 = hi16(s23);
      float kf[7];
#pragma unroll
      for (int i = 0; i < 7; ++i) kf[i] = (j & 1) ? hi16(kk[i][j >> 1]) : lo16(kk[i][j >> 1]);
      float r[4];
#pragma unroll
      for (int i = 0; i < 4; ++i) {
        float a = kf[i] * s3; a = fmaf(kf[i + 1], s2, a); a = fmaf(kf[i + 2], s1, a); a = fmaf(kf[i + 3], s0, a);
        const float vf = (j & 1) ? hi16(vv[i][j >> 1]) : lo16(vv[i][j >> 1]);
        r[i] = a * vf;
      }
      o[j] = u32x2{pk2(r[3], r[2]), pk2(r[1], r[0])};
    }
    {
      LAS char* stg = (LAS char*)smem + G1_WBASE;
      const int pos = 124 - (wid * 32 + tg * 4);
#pragma unroll
      for (int j = 0; j < 8; ++j) *(LAS u32x2*)(stg + (cgp * 8 + j) * 272 + cgp * 16 + pos * 2) = o[j];
      asm volatile("s_waitcnt lgkmcnt(0)" ::: "memory");
      BARRIER();
#pragma unroll
      for (int i = 0; i < 4; ++i) {
        const int row = wid * 16 + i * 4 + (lane >> 4);
        const u32x4 w = *(const LAS u32x4*)(stg + row * 272 + (row >> 3) * 16 + (lane & 15) * 16);
        st16_dev(kvr + (size_t)(bc * 64 + row) * KVP + (1920 - bt * 128) + (lane & 15) * 8, w);
      }
    }
    asm volatile("s_waitcnt vmcnt(0)" : "+v"(epoch), "+v"(cpf) :: "memory");
    tag = (((u64)(u32)__builtin_amdgcn_readfirstlane(epoch[1]) << 32) | (u64)(u32)__builtin_amdgcn_readfirstlane(epoch[0])) + 1;
    BARRIER();
    if (tid == 0) signal_flag(flag0 + bc * 16 + bt, tag);
    if (wid != 0) {
      const auto srdK = mk_rsrc(ssmk, D_ * S_ * 2);
#pragma unroll
      for (int rep = 0; rep < 2; ++rep) {
        if (rep == 1 && wid != 1) break;
        const int tw = rep == 0 ? wid : 0;
        const int vo = ((bid * 4 + tw) * S_ + 8 * lane) * 2;
        asm volatile(
      "s_nop 4\n\t"
      "s_add_u32 m0, %[lds], 32\n\t"
      "s_nop 0\n\t"
      "buffer_load_dwordx4 %[v0], %[srdK], 0 offen nt lds\n\t"
      "s_add_u32 m0, %[lds], 1056\n\t"
      "s_nop 0\n\t"
      "buffer_load_dwordx4 %[v0], %[srdK], %[s1] offen nt lds\n\t"
      "s_add_u32 m0, %[lds], 2080\n\t"
      "s_nop 0\n\t"
      "buffer_load_dwordx4 %[v0], %[srdK], %[s2] offen nt lds\n\t"
      "s_add_u32 m0, %[lds], 3104\n\t"
      "s_nop 0\n\t"
      "buffer_load_dwordx4 %[v0], %[srdK], %[s3] offen nt lds\n\t"
      "s_add_u32 m0, %[lds], 4192\n\t"
      "s_nop 0\n\t"
      "buffer_load_dwordx4 %[v1], %[srdK], 0 offen nt lds\n\t"
      "s_add_u32 m0, %[lds], 5216\n\t"
      "s_nop 0\n\t"
      "buffer_load_dwordx4 %[v1], %[srdK], %[s1] offen nt lds\n\t"
      "s_add_u32 m0, %[lds], 6240\n\t"
      "s_nop 0\n\t"
      "buffer_load_dwordx4 %[v1], %[srdK], %[s2] offen nt lds\n\t"
      "s_add_u32 m0, %[lds], 7264\n\t"
      "s_nop 0\n\t"
      "buffer_load_dwordx4 %[v1], %[srdK], %[s3] offen nt lds\n\t"
            :: [lds] "s"(__builtin_amdgcn_readfirstlane(sbase + tw * WSTR)), [v0] "v"(vo), [v1] "v"(vo + 2), [srdK] "s"(srdK), [s1] "s"(1024), [s2] "s"(2048), [s3] "s"(3072) : "memory", "scc");
      }
    }
  }

  {
    {
      if (wid == 0) poll_flags(flag0 + (bid >> 4) * 16, 16, tag, lane);
      BARRIER();
      const auto srdX = mk_rsrc(kvr, D_ * KVP * 2);
      const int vo = (ch * KVP + 8 * lane) * 2;
      asm volatile(
      "s_nop 4\n\t"
      "s_add_u32 m0, %[lds], 8320\n\t"
      "s_nop 0\n\t"
      "buffer_load_dwordx4 %[v0], %[srdX], 0 offen sc1 lds\n\t"
      "s_add_u32 m0, %[lds], 9344\n\t"
      "s_nop 0\n\t"
      "buffer_load_dwordx4 %[v0], %[srdX], %[s1] offen sc1 lds\n\t"
      "s_add_u32 m0, %[lds], 10368\n\t"
      "s_nop 0\n\t"
      "buffer_load_dwordx4 %[v0], %[srdX], %[s2] offen sc1 lds\n\t"
      "s_add_u32 m0, %[lds], 11392\n\t"
      "s_nop 0\n\t"
      "buffer_load_dwordx4 %[v0], %[srdX], %[s3] offen sc1 lds\n\t"
          :: [lds] "s"(__builtin_amdgcn_readfirstlane(sbase + wid * WSTR)), [v0] "v"(vo), [srdX] "s"(srdX), [s1] "s"(1024), [s2] "s"(2048), [s3] "s"(3072) : "memory", "scc");
      const u16* xr = x + (size_t)(bid * 8 + 2 * wid) * D_ + 8 * lane;
      asm volatile("global_load_dwordx4 %0, %4, off nt\n\tglobal_load_dwordx4 %1, %4, off offset:1024 nt\n\tglobal_load_dwordx4 %2, %4, off offset:2048 nt\n\tglobal_load_dwordx4 %3, %4, off offset:3072 nt"
                   : "=&v"(xv[0][0]), "=&v"(xv[0][1]), "=&v"(xv[1][0]), "=&v"(xv[1][1]) : "v"(xr) : "memory");
      const auto srdS = mk_rsrc((const char*)xbuf + (size_t)bid * 16384, 16384);
      const u32x4 ones = {0xffffffffu, 0xffffffffu, 0xffffffffu, 0xffffffffu};
      asm volatile("buffer_store_dwordx4 %0, %1, %2, 0 offen sc1\n\tbuffer_store_dwordx4 %0, %1, %2, 0 offen offset:1024 sc1\n\tbuffer_store_dwordx4 %0, %1, %2, 0 offen offset:2048 sc1\n\tbuffer_store_dwordx4 %0, %1, %2, 0 offen offset:3072 sc1\n\ts_nop 1"
                   :: "v"(ones), "v"(wid * 4096 + lane * 16), "s"(srdS) : "memory");
      asm volatile("s_waitcnt vmcnt(8) lgkmcnt(0)" ::: "memory");
      BARRIER();
    }
    u32 pfd = 0;
    {
      const size_t pa = (size_t)((wid & 1) ? wu : wg);
      const u32 plo = (u32)__builtin_amdgcn_readfirstlane((u32)pa), phi = (u32)__builtin_amdgcn_readfirstlane((u32)(pa >> 32));
      constexpr u32 per = 320 * 56 / 512;
      prefetch_256_lines((const void*)(((size_t)phi << 32) | (size_t)plo), (bid * 2 + (wid >> 1)) * per, 320 * 56, lane, pfd, per);
    }
    const int j = lane & 15, q = lane >> 4;
    const u32 wbase = (u32)(size_t)wb;
    f32x32 acc;
    {
      const int c = (j + 1) & 1;
      const u32 ab = wbase + c * CSTR + 16 * q + 2 * (j + 1 - c);
      u32x8 ad = {ab, ab + 1024, ab + 2048, ab + 3072, wbase + XOFF + 2 * (2032 - 16 * j + 8 * q) - 512 * 7, 0, 0, 0};
      asm volatile(
      "s_nop 1\n\t"
      "ds_read2_b32 a[0:1], v32 offset0:0 offset1:1\n\t"
      "ds_read2_b32 a[2:3], v32 offset0:2 offset1:3\n\t"
      "ds_read_b128 a[32:35], v36 offset:3584\n\t"
      "ds_read2_b32 a[4:5], v32 offset0:128 offset1:129\n\t"
      "ds_read2_b32 a[6:7], v32 offset0:130 offset1:131\n\t"
      "ds_read_b128 a[36:39], v36 offset:3072\n\t"
      "ds_read2_b32 a[8:9], v33 offset0:0 offset1:1\n\t"
      "ds_read2_b32 a[10:11], v33 offset0:2 offset1:3\n\t"
      "ds_read_b128 a[40:43], v36 offset:2560\n\t"
      "ds_read2_b32 a[12:13], v33 offset0:128 offset1:129\n\t"
      "ds_read2_b32 a[14:15], v33 offset0:130 offset1:131\n\t"
      "ds_read_b128 a[44:47], v36 offset:2048\n\t"
      "ds_read2_b32 a[16:17], v34 offset0:0 offset1:1\n\t"
      "ds_read2_b32 a[18:19], v34 offset0:2 offset1:3\n\t"
      "ds_read_b128 a[48:51], v36 offset:1536\n\t"
      "ds_read2_b32 a[20:21], v34 offset0:128 offset1:129\n\t"
      "ds_read2_b32 a[22:23], v34 offset0:130 offset1:131\n\t"
      "ds_read_b128 a[52:55], v36 offset:1024\n\t"
      "ds_read2_b32 a[24:25], v35 offset0:0 offset1:1\n\t"
      "ds_read2_b32 a[26:27], v35 offset0:2 offset1:3\n\t"
      "ds_read_b128 a[56:59], v36 offset:512\n\t"
      "ds_read2_b32 a[28:29], v35 offset0:128 offset1:129\n\t"
      "ds_read2_b32 a[30:31], v35 offset0:130 offset1:131\n\t"
      "ds_read_b128 a[60:63], v36 offset:0\n\t"
      "v_mov_b32 v0, 0\n\t"
      "v_mov_b32 v1, 0\n\t"
      "v_mov_b32 v2, 0\n\t"
      "v_mov_b32 v3, 0\n\t"
      "v_mov_b32 v4, 0\n\t"
      "v_mov_b32 v5, 0\n\t"
      "v_mov_b32 v6, 0\n\t"
      "v_mov_b32 v7, 0\n\t"
      "v_mov_b32 v8, 0\n\t"
      "v_mov_b32 v9, 0\n\t"
      "v_mov_b32 v10, 0\n\t"
      "v_mov_b32 v11, 0\n\t"
      "v_mov_b32 v12, 0\n\t"
      "v_mov_b32 v13, 0\n\t"
      "v_mov_b32 v14, 0\n\t"
      "v_mov_b32 v15, 0\n\t"
      "v_mov_b32 v16, 0\n\t"
      "v_mov_b32 v17, 0\n\t"
      "v_mov_b32 v18, 0\n\t"
      "v_mov_b32 v19, 0\n\t"
      "v_mov_b32 v20, 0\n\t"
      "v_mov_b32 v21, 0\n\t"
      "v_mov_b32 v22, 0\n\t"
      "v_mov_b32 v23, 0\n\t"
      "v_mov_b32 v24, 0\n\t"
      "v_mov_b32 v25, 0\n\t"
      "v_mov_b32 v26, 0\n\t"
      "v_mov_b32 v27, 0\n\t"
      "v_mov_b32 v28, 0\n\t"
      "v_mov_b32 v29, 0\n\t"
      "v_mov_b32 v30, 0\n\t"
      "v_mov_b32 v31, 0\n\t"
      "s_waitcnt lgkmcnt(0)\n\t"
      "v_mfma_f32_16x16x32_bf16 v[0:3], a[0:3], a[32:35], v[0:3]\n\t"
      "ds_read2_b32 a[64:65], v32 offset0:16 offset1:17\n\t"
      "v_mfma_f32_16x16x32_bf16 v[4:7], a[0:3], a[36:39], v[4:7]\n\t"
      "ds_read2_b32 a[66:67], v32 offset0:18 offset1:19\n\t"
      "v_mfma_f32_16x16x32_bf16 v[4:7], a[4:7], a[32:35], v[4:7]\n\t"
      "ds_read_b128 a[96:99], v36 offset:3648\n\t"
      "v_mfma_f32_16x16x32_bf16 v[8:11], a[0:3], a[40:43], v[8:11]\n\t"
      "ds_read2_b32 a[68:69], v32 offset0:144 offset1:145\n\t"
      "v_mfma_f32_16x16x32_bf16 v[8:11], a[4:7], a[36:39], v[8:11]\n\t"
      "ds_read2_b32 a[70:71], v32 offset0:146 offset1:147\n\t"
      "v_mfma_f32_16x16x32_bf16 v[8:11], a[8:11], a[32:35], v[8:11]\n\t"
      "ds_read_b128 a[100:103], v36 offset:3136\n\t"
      "v_mfma_f32_16x16x32_bf16 v[12:15], a[0:3], a[44:47], v[12:15]\n\t"
      "ds_read2_b32 a[72:73], v33 offset0:16 offset1:17\n\t"
      "v_mfma_f32_16x16x32_bf16 v[12:15], a[4:7], a[40:43], v[12:15]\n\t"
      "ds_read2_b32 a[74:75], v33 offset0:18 offset1:19\n\t"
      "v_mfma_f32_16x16x32_bf16 v[12:15], a[8:11], a[36:39], v[12:15]\n\t"
      "ds_read_b128 a[104:107], v36 offset:2624\n\t"
      "v_mfma_f32_16x16x32_bf16 v[12:15], a[12:15], a[32:35], v[12:15]\n\t"
      "ds_read2_b32 a[76:77], v33 offset0:144 offset1:145\n\t"
      "v_mfma_f32_16x16x32_bf16 v[16:19], a[0:3], a[48:51], v[16:19]\n\t"
      "ds_read2_b32 a[78:79], v33 offset0:146 offset1:147\n\t"
      "v_mfma_f32_16x16x32_bf16 v[16:19], a[4:7], a[44:47], v[16:19]\n\t"
      "ds_read_b128 a[108:111], v36 offset:2112\n\t"
      "v_mfma_f32_16x16x32_bf16 v[16:19], a[8:11], a[40:43], v[16:19]\n\t"
      "ds_read2_b32 a[80:81], v34 offset0:16 offset1:17\n\t"
      "v_mfma_f32_16x16x32_bf16 v[16:19], a[12:15], a[36:39], v[16:19]\n\t"
      "ds_read2_b32 a[82:83], v34 offset0:18 offset1:19\n\t"
      "v_mfma_f32_16x16x32_bf16 v[16:19], a[16:19], a[32:35], v[16:19]\n\t"
      "ds_read_b128 a[112:115], v36 offset:1600\n\t"
      "v_mfma_f32_16x16x32_bf16 v[20:23], a[0:3], a[52:55], v[20:23]\n\t"
      "ds_read2_b32 a[84:85], v34 offset0:144 offset1:145\n\t"
      "v_mfma_f32_16x16x32_bf16 v[20:23], a[4:7], a[48:51], v[20:23]\n\t"
      "ds_read2_b32 a[86:87], v34 offset0:146 offset1:147\n\t"
      "v_mfma_f32_16x16x32_bf16 v[20:23], a[8:11], a[44:47], v[20:23]\n\t"
      "ds_read_b128 a[116:119], v36 offset:1088\n\t"
      "v_mfma_f32_16x16x32_bf16 v[20:23], a[12:15], a[40:43], v[20:23]\n\t"
      "ds_read2_b32 a[88:89], v35 offset0:16 offset1:17\n\t"
      "v_mfma_f32_16x16x32_bf16 v[20:23], a[16:19], a[36:39], v[20:23]\n\t"
      "ds_read2_b32 a[90:91], v35 offset0:18 offset1:19\n\t"
      "v_mfma_f32_16x16x32_bf16 v[20:23], a[20:23], a[32:35], v[20:23]\n\t"
      "ds_read_b128 a[120:123], v36 offset:576\n\t"
      "v_mfma_f32_16x16x32_bf16 v[24:27], a[0:3], a[56:59], v[24:27]\n\t"
      "ds_read2_b32 a[92:93], v35 offset0:144 offset1:145\n\t"
      "v_mfma_f32_16x16x32_bf16 v[24:27], a[4:7], a[52:55], v[24:27]\n\t"
      "ds_read2_b32 a[94:95], v35 offset0:146 offset1:147\n\t"
      "v_mfma_f32_16x16x32_bf16 v[24:27], a[8:11], a[48:51], v[24:27]\n\t"
      "ds_read_b128 a[124:127], v36 offset:64\n\t"
      "v_mfma_f32_16x16x32_bf16 v[24:27], a[12:15], a[44:47], v[24:27]\n\t"
      "v_mfma_f32_16x16x32_bf16 v[24:27], a[16:19], a[40:43], v[24:27]\n\t"
      "v_mfma_f32_16x16x32_bf16 v[24:27], a[20:23], a[36:39], v[24:27]\n\t"
      "v_mfma_f32_16x16x32_bf16 v[24:27], a[24:27], a[32:35], v[24:27]\n\t"
      "v_mfma_f32_16x16x32_bf16 v[28:31], a[0:3], a[60:63], v[28:31]\n\t"
      "v_mfma_f32_16x16x32_bf16 v[28:31], a[4:7], a[56:59], v[28:31]\n\t"
      "v_mfma_f32_16x16x32_bf16 v[28:31], a[8:11], a[52:55], v[28:31]\n\t"
      "v_mfma_f32_16x16x32_bf16 v[28:31], a[12:15], a[48:51], v[28:31]\n\t"
      "v_mfma_f32_16x16x32_bf16 v[28:31], a[16:19], a[44:47], v[28:31]\n\t"
      "v_mfma_f32_16x16x32_bf16 v[28:31], a[20:23], a[40:43], v[28:31]\n\t"
      "v_mfma_f32_16x16x32_bf16 v[28:31], a[24:27], a[36:39], v[28:31]\n\t"
      "v_mfma_f32_16x16x32_bf16 v[28:31], a[28:31], a[32:35], v[28:31]\n\t"
      "s_waitcnt lgkmcnt(0)\n\t"
      "v_mfma_f32_16x16x32_bf16 v[0:3], a[64:67], a[96:99], v[0:3]\n\t"
      "ds_read2_b32 a[0:1], v32 offset0:32 offset1:33\n\t"
      "v_mfma_f32_16x16x32_bf16 v[4:7], a[64:67], a[100:103], v[4:7]\n\t"
      "ds_read2_b32 a[2:3], v32 offset0:34 offset1:35\n\t"
      "v_mfma_f32_16x16x32_bf16 v[4:7], a[68:71], a[96:99], v[4:7]\n\t"
      "ds_read_b128 a[32:35], v36 offset:3712\n\t"
      "v_mfma_f32_16x16x32_bf16 v[8:11], a[64:67], a[104:107], v[8:11]\n\t"
      "ds_read2_b32 a[4:5], v32 offset0:160 offset1:161\n\t"
      "v_mfma_f32_16x16x32_bf16 v[8:11], a[68:71], a[100:103], v[8:11]\n\t"
      "ds_read2_b32 a[6:7], v32 offset0:162 offset1:163\n\t"
      "v_mfma_f32_16x16x32_bf16 v[8:11], a[72:75], a[96:99], v[8:11]\n\t"
      "ds_read_b128 a[36:39], v36 offset:3200\n\t"
      "v_mfma_f32_16x16x32_bf16 v[12:15], a[64:67], a[108:111], v[12:15]\n\t"
      "ds_read2_b32 a[8:9], v33 offset0:32 offset1:33\n\t"
      "v_mfma_f32_16x16x32_bf16 v[12:15], a[68:71], a[104:107], v[12:15]\n\t"
      "ds_read2_b32 a[10:11], v33 offset0:34 offset1:35\n\t"
      "v_mfma_f32_16x16x32_bf16 v[12:15], a[72:75], a[100:103], v[12:15]\n\t"
      "ds_read_b128 a[40:43], v36 offset:2688\n\t"
      "v_mfma_f32_16x16x32_bf16 v[12:15], a[76:79], a[96:99], v[12:15]\n\t"
      "ds_read2_b32 a[12:13], v33 offset0:160 offset1:161\n\t"
      "v_mfma_f32_16x16x32_bf16 v[16:19], a[64:67], a[112:115], v[16:19]\n\t"
      "ds_read2_b32 a[14:15], v33 offset0:162 offset1:163\n\t"
      "v_mfma_f32_16x16x32_bf16 v[16:19], a[68:71], a[108:111], v[16:19]\n\t"
      "ds_read_b128 a[44:47], v36 offset:2176\n\t"
      "v_mfma_f32_16x16x32_bf16 v[16:19], a[72:75], a[104:107], v[16:19]\n\t"
      "ds_read2_b32 a[16:17], v34 offset0:32 offset1:33\n\t"
      "v_mfma_f32_16x16x32_bf16 v[16:19], a[76:79], a[100:103], v[16:19]\n\t"
      "ds_read2_b32 a[18:19], v34 offset0:34 offset1:35\n\t"
      "v_mfma_f32_16x16x32_bf16 v[16:19], a[80:83], a[96:99], v[16:19]\n\t"
      "ds_read_b128 a[48:51], v36 offset:1664\n\t"
      "v_mfma_f32_16x16x32_bf16 v[20:23], a[64:67], a[116:119], v[20:23]\n\t"
      "ds_read2_b32 a[20:21], v34 offset0:160 offset1:161\n\t"
      "v_mfma_f32_16x16x32_bf16 v[20:23], a[68:71], a[112:115], v[20:23]\n\t"
      "ds_read2_b32 a[22:23], v34 offset0:162 offset1:163\n\t"
      "v_mfma_f32_16x16x32_bf16 v[20:23], a[72:75], a[108:111], v[20:23]\n\t"
      "ds_read_b128 a[52:55], v36 offset:1152\n\t"
      "v_mfma_f32_16x16x32_bf16 v[20:23], a[76:79], a[104:107], v[20:23]\n\t"
      "ds_read2_b32 a[24:25], v35 offset0:32 offset1:33\n\t"
      "v_mfma_f32_16x16x32_bf16 v[20:23], a[80:83], a[100:103], v[20:23]\n\t"
      "ds_read2_b32 a[26:27], v35 offset0:34 offset1:35\n\t"
      "v_mfma_f32_16x16x32_bf16 v[20:23], a[84:87], a[96:99], v[20:23]\n\t"
      "ds_read_b128 a[56:59], v36 offset:640\n\t"
      "v_mfma_f32_16x16x32_bf16 v[24:27], a[64:67], a[120:123], v[24:27]\n\t"
      "ds_read2_b32 a[28:29], v35 offset0:160 offset1:161\n\t"
      "v_mfma_f32_16x16x32_bf16 v[24:27], a[68:71], a[116:119], v[24:27]\n\t"
      "ds_read2_b32 a[30:31], v35 offset0:162 offset1:163\n\t"
      "v_mfma_f32_16x16x32_bf16 v[24:27], a[72:75], a[112:115], v[24:27]\n\t"
      "ds_read_b128 a[60:63], v36 offset:128\n\t"
      "v_mfma_f32_16x16x32_bf16 v[24:27], a[76:79], a[108:111], v[24:27]\n\t"
      "v_mfma_f32_16x16x32_bf16 v[24:27], a[80:83], a[104:107], v[24:27]\n\t"
      "v_mfma_f32_16x16x32_bf16 v[24:27], a[84:87], a[100:103], v[24:27]\n\t"
      "v_mfma_f32_16x16x32_bf16 v[24:27], a[88:91], a[96:99], v[24:27]\n\t"
      "v_mfma_f32_16x16x32_bf16 v[28:31], a[64:67], a[124:127], v[28:31]\n\t"
      "v_mfma_f32_16x16x32_bf16 v[28:31], a[68:71], a[120:123], v[28:31]\n\t"
      "v_mfma_f32_16x16x32_bf16 v[28:31], a[72:75], a[116:119], v[28:31]\n\t"
      "v_mfma_f32_16x16x32_bf16 v[28:31], a[76:79], a[112:115], v[28:31]\n\t"
      "v_mfma_f32_16x16x32_bf16 v[28:31], a[80:83], a[108:111], v[28:31]\n\t"
      "v_mfma_f32_16x16x32_bf16 v[28:31], a[84:87], a[104:107], v[28:31]\n\t"
      "v_mfma_f32_16x16x32_bf16 v[28:31], a[88:91], a[100:103], v[28:31]\n\t"
      "v_mfma_f32_16x16x32_bf16 v[28:31], a[92:95], a[96:99], v[28:31]\n\t"
      "s_waitcnt lgkmcnt(0)\n\t"
      "v_mfma_f32_16x16x32_bf16 v[0:3], a[0:3], a[32:35], v[0:3]\n\t"
      "ds_read2_b32 a[64:65], v32 offset0:48 offset1:49\n\t"
      "v_mfma_f32_16x16x32_bf16 v[4:7], a[0:3], a[36:39], v[4:7]\n\t"
      "ds_read2_b32 a[66:67], v32 offset0:50 offset1:51\n\t"
      "v_mfma_f32_16x16x32_bf16 v[4:7], a[4:7], a[32:35], v[4:7]\n\t"
      "ds_read_b128 a[96:99], v36 offset:3776\n\t"
      "v_mfma_f32_16x16x32_bf16 v[8:11], a[0:3], a[40:43], v[8:11]\n\t"
      "ds_read2_b32 a[68:69], v32 offset0:176 offset1:177\n\t"
      "v_mfma_f32_16x16x32_bf16 v[8:11], a[4:7], a[36:39], v[8:11]\n\t"
      "ds_read2_b32 a[70:71], v32 offset0:178 offset1:179\n\t"
      "v_mfma_f32_16x16x32_bf16 v[8:11], a[8:11], a[32:35], v[8:11]\n\t"
      "ds_read_b128 a[100:103], v36 offset:3264\n\t"
      "v_mfma_f32_16x16x32_bf16 v[12:15], a[0:3], a[44:47], v[12:15]\n\t"
      "ds_read2_b32 a[72:73], v33 offset0:48 offset1:49\n\t"
      "v_mfma_f32_16x16x32_bf16 v[12:15], a[4:7], a[40:43], v[12:15]\n\t"
      "ds_read2_b32 a[74:75], v33 offset0:50 offset1:51\n\t"
      "v_mfma_f32_16x16x32_bf16 v[12:15], a[8:11], a[36:39], v[12:15]\n\t"
      "ds_read_b128 a[104:107], v36 offset:2752\n\t"
      "v_mfma_f32_16x16x32_bf16 v[12:15], a[12:15], a[32:35], v[12:15]\n\t"
      "ds_read2_b32 a[76:77], v33 offset0:176 offset1:177\n\t"
      "v_mfma_f32_16x16x32_bf16 v[16:19], a[0:3], a[48:51], v[16:19]\n\t"
      "ds_read2_b32 a[78:79], v33 offset0:178 offset1:179\n\t"
      "v_mfma_f32_16x16x32_bf16 v[16:19], a[4:7], a[44:47], v[16:19]\n\t"
      "ds_read_b128 a[108:111], v36 offset:2240\n\t"
      "v_mfma_f32_16x16x32_bf16 v[16:19], a[8:11], a[40:43], v[16:19]\n\t"
      "ds_read2_b32 a[80:81], v34 offset0:48 offset1:49\n\t"
      "v_mfma_f32_16x16x32_bf16 v[16:19], a[12:15], a[36:39], v[16:19]\n\t"
      "ds_read2_b32 a[82:83], v34 offset0:50 offset1:51\n\t"
      "v_mfma_f32_16x16x32_bf16 v[16:19], a[16:19], a[32:35], v[16:19]\n\t"
      "ds_read_b128 a[112:115], v36 offset:1728\n\t"
      "v_mfma_f32_16x16x32_bf16 v[20:23], a[0:3], a[52:55], v[20:23]\n\t"
      "ds_read2_b32 a[84:85], v34 offset0:176 offset1:177\n\t"
      "v_mfma_f32_16x16x32_bf16 v[20:23], a[4:7], a[48:51], v[20:23]\n\t"
      "ds_read2_b32 a[86:87], v34 offset0:178 offset1:179\n\t"
      "v_mfma_f32_16x16x32_bf16 v[20:23], a[8:11], a[44:47], v[20:23]\n\t"
      "ds_read_b128 a[116:119], v36 offset:1216\n\t"
      "v_mfma_f32_16x16x32_bf16 v[20:23], a[12:15], a[40:43], v[20:23]\n\t"
      "ds_read2_b32 a[88:89], v35 offset0:48 offset1:49\n\t"
      "v_mfma_f32_16x16x32_bf16 v[20:23], a[16:19], a[36:39], v[20:23]\n\t"
      "ds_read2_b32 a[90:91], v35 offset0:50 offset1:51\n\t"
      "v_mfma_f32_16x16x32_bf16 v[20:23], a[20:23], a[32:35], v[20:23]\n\t"
      "ds_read_b128 a[120:123], v36 offset:704\n\t"
      "v_mfma_f32_16x16x32_bf16 v[24:27], a[0:3], a[56:59], v[24:27]\n\t"
      "ds_read2_b32 a[92:93], v35 offset0:176 offset1:177\n\t"
      "v_mfma_f32_16x16x32_bf16 v[24:27], a[4:7], a[52:55], v[24:27]\n\t"
      "ds_read2_b32 a[94:95], v35 offset0:178 offset1:179\n\t"
      "v_mfma_f32_16x16x32_bf16 v[24:27], a[8:11], a[48:51], v[24:27]\n\t"
      "ds_read_b128 a[124:127], v36 offset:192\n\t"
      "v_mfma_f32_16x16x32_bf16 v[24:27], a[12:15], a[44:47], v[24:27]\n\t"
      "v_mfma_f32_16x16x32_bf16 v[24:27], a[16:19], a[40:43], v[24:27]\n\t"
      "v_mfma_f32_16x16x32_bf16 v[24:27], a[20:23], a[36:39], v[24:27]\n\t"
      "v_mfma_f32_16x16x32_bf16 v[24:27], a[24:27], a[32:35], v[24:27]\n\t"
      "v_mfma_f32_16x16x32_bf16 v[28:31], a[0:3], a[60:63], v[28:31]\n\t"
      "v_mfma_f32_16x16x32_bf16 v[28:31], a[4:7], a[56:59], v[28:31]\n\t"
      "v_mfma_f32_16x16x32_bf16 v[28:31], a[8:11], a[52:55], v[28:31]\n\t"
      "v_mfma_f32_16x16x32_bf16 v[28:31], a[12:15], a[48:51], v[28:31]\n\t"
      "v_mfma_f32_16x16x32_bf16 v[28:31], a[16:19], a[44:47], v[28:31]\n\t"
      "v_mfma_f32_16x16x32_bf16 v[28:31], a[20:23], a[40:43], v[28:31]\n\t"
      "v_mfma_f32_16x16x32_bf16 v[28:31], a[24:27], a[36:39], v[28:31]\n\t"
      "v_mfma_f32_16x16x32_bf16 v[28:31], a[28:31], a[32:35], v[28:31]\n\t"
      "s_waitcnt lgkmcnt(0)\n\t"
      "v_mfma_f32_16x16x32_bf16 v[0:3], a[64:67], a[96:99], v[0:3]\n\t"
      "ds_read2_b32 a[0:1], v32 offset0:64 offset1:65\n\t"
      "v_mfma_f32_16x16x32_bf16 v[4:7], a[64:67], a[100:103], v[4:7]\n\t"
      "ds_read2_b32 a[2:3], v32 offset0:66 offset1:67\n\t"
      "v_mfma_f32_16x16x32_bf16 v[4:7], a[68:71], a[96:99], v[4:7]\n\t"
      "ds_read_b128 a[32:35], v36 offset:3840\n\t"
      "v_mfma_f32_16x16x32_bf16 v[8:11], a[64:67], a[104:107], v[8:11]\n\t"
      "ds_read2_b32 a[4:5], v32 offset0:192 offset1:193\n\t"
      "v_mfma_f32_16x16x32_bf16 v[8:11], a[68:71], a[100:103], v[8:11]\n\t"
      "ds_read2_b32 a[6:7], v32 offset0:194 offset1:195\n\t"
      "v_mfma_f32_16x16x32_bf16 v[8:11], a[72:75], a[96:99], v[8:11]\n\t"
      "ds_read_b128 a[36:39], v36 offset:3328\n\t"
      "v_mfma_f32_16x16x32_bf16 v[12:15], a[64:67], a[108:111], v[12:15]\n\t"
      "ds_read2_b32 a[8:9], v33 offset0:64 offset1:65\n\t"
      "v_mfma_f32_16x16x32_bf16 v[12:15], a[68:71], a[104:107], v[12:15]\n\t"
      "ds_read2_b32 a[10:11], v33 offset0:66 offset1:67\n\t"
      "v_mfma_f32_16x16x32_bf16 v[12:15], a[72:75], a[100:103], v[12:15]\n\t"
      "ds_read_b128 a[40:43], v36 offset:2816\n\t"
      "v_mfma_f32_16x16x32_bf16 v[12:15], a[76:79], a[96:99], v[12:15]\n\t"
      "ds_read2_b32 a[12:13], v33 offset0:192 offset1:193\n\t"
      "v_mfma_f32_16x16x32_bf16 v[16:19], a[64:67], a[112:115], v[16:19]\n\t"
      "ds_read2_b32 a[14:15], v33 offset0:194 offset1:195\n\t"
      "v_mfma_f32_16x16x32_bf16 v[16:19], a[68:71], a[108:111], v[16:19]\n\t"
      "ds_read_b128 a[44:47], v36 offset:2304\n\t"
      "v_mfma_f32_16x16x32_bf16 v[16:19], a[72:75], a[104:107], v[16:19]\n\t"
      "ds_read2_b32 a[16:17], v34 offset0:64 offset1:65\n\t"
      "v_mfma_f32_16x16x32_bf16 v[16:19], a[76:79], a[100:103], v[16:19]\n\t"
      "ds_read2_b32 a[18:19], v34 offset0:66 offset1:67\n\t"
      "v_mfma_f32_16x16x32_bf16 v[16:19], a[80:83], a[96:99], v[16:19]\n\t"
      "ds_read_b128 a[48:51], v36 offset:1792\n\t"
      "v_mfma_f32_16x16x32_bf16 v[20:23], a[64:67], a[116:119], v[20:23]\n\t"
      "ds_read2_b32 a[20:21], v34 offset0:192 offset1:193\n\t"
      "v_mfma_f32_16x16x32_bf16 v[20:23], a[68:71], a[112:115], v[20:23]\n\t"
      "ds_read2_b32 a[22:23], v34 offset0:194 offset1:195\n\t"
      "v_mfma_f32_16x16x32_bf16 v[20:23], a[72:75], a[108:111], v[20:23]\n\t"
      "ds_read_b128 a[52:55], v36 offset:1280\n\t"
      "v_mfma_f32_16x16x32_bf16 v[20:23], a[76:79], a[104:107], v[20:23]\n\t"
      "ds_read2_b32 a[24:25], v35 offset0:64 offset1:65\n\t"
      "v_mfma_f32_16x16x32_bf16 v[20:23], a[80:83], a[100:103], v[20:23]\n\t"
      "ds_read2_b32 a[26:27], v35 offset0:66 offset1:67\n\t"
      "v_mfma_f32_16x16x32_bf16 v[20:23], a[84:87], a[96:99], v[20:23]\n\t"
      "ds_read_b128 a[56:59], v36 offset:768\n\t"
      "v_mfma_f32_16x16x32_bf16 v[24:27], a[64:67], a[120:123], v[24:27]\n\t"
      "ds_read2_b32 a[28:29], v35 offset0:192 offset1:193\n\t"
      "v_mfma_f32_16x16x32_bf16 v[24:27], a[68:71], a[116:119], v[24:27]\n\t"
      "ds_read2_b32 a[30:31], v35 offset0:194 offset1:195\n\t"
      "v_mfma_f32_16x16x32_bf16 v[24:27], a[72:75], a[112:115], v[24:27]\n\t"
      "ds_read_b128 a[60:63], v36 offset:256\n\t"
      "v_mfma_f32_16x16x32_bf16 v[24:27], a[76:79], a[108:111], v[24:27]\n\t"
      "v_mfma_f32_16x16x32_bf16 v[24:27], a[80:83], a[104:107], v[24:27]\n\t"
      "v_mfma_f32_16x16x32_bf16 v[24:27], a[84:87], a[100:103], v[24:27]\n\t"
      "v_mfma_f32_16x16x32_bf16 v[24:27], a[88:91], a[96:99], v[24:27]\n\t"
      "v_mfma_f32_16x16x32_bf16 v[28:31], a[64:67], a[124:127], v[28:31]\n\t"
      "v_mfma_f32_16x16x32_bf16 v[28:31], a[68:71], a[120:123], v[28:31]\n\t"
      "v_mfma_f32_16x16x32_bf16 v[28:31], a[72:75], a[116:119], v[28:31]\n\t"
      "v_mfma_f32_16x16x32_bf16 v[28:31], a[76:79], a[112:115], v[28:31]\n\t"
      "v_mfma_f32_16x16x32_bf16 v[28:31], a[80:83], a[108:111], v[28:31]\n\t"
      "v_mfma_f32_16x16x32_bf16 v[28:31], a[84:87], a[104:107], v[28:31]\n\t"
      "v_mfma_f32_16x16x32_bf16 v[28:31], a[88:91], a[100:103], v[28:31]\n\t"
      "v_mfma_f32_16x16x32_bf16 v[28:31], a[92:95], a[96:99], v[28:31]\n\t"
      "s_waitcnt lgkmcnt(0)\n\t"
      "v_mfma_f32_16x16x32_bf16 v[0:3], a[0:3], a[32:35], v[0:3]\n\t"
      "ds_read2_b32 a[64:65], v32 offset0:80 offset1:81\n\t"
      "v_mfma_f32_16x16x32_bf16 v[4:7], a[0:3], a[36:39], v[4:7]\n\t"
      "ds_read2_b32 a[66:67], v32 offset0:82 offset1:83\n\t"
      "v_mfma_f32_16x16x32_bf16 v[4:7], a[4:7], a[32:35], v[4:7]\n\t"
      "ds_read_b128 a[96:99], v36 offset:3904\n\t"
      "v_mfma_f32_16x16x32_bf16 v[8:11], a[0:3], a[40:43], v[8:11]\n\t"
      "ds_read2_b32 a[68:69], v32 offset0:208 offset1:209\n\t"
      "v_mfma_f32_16x16x32_bf16 v[8:11], a[4:7], a[36:39], v[8:11]\n\t"
      "ds_read2_b32 a[70:71], v32 offset0:210 offset1:211\n\t"
      "v_mfma_f32_16x16x32_bf16 v[8:11], a[8:11], a[32:35], v[8:11]\n\t"
      "ds_read_b128 a[100:103], v36 offset:3392\n\t"
      "v_mfma_f32_16x16x32_bf16 v[12:15], a[0:3], a[44:47], v[12:15]\n\t"
      "ds_read2_b32 a[72:73], v33 offset0:80 offset1:81\n\t"
      "v_mfma_f32_16x16x32_bf16 v[12:15], a[4:7], a[40:43], v[12:15]\n\t"
      "ds_read2_b32 a[74:75], v33 offset0:82 offset1:83\n\t"
      "v_mfma_f32_16x16x32_bf16 v[12:15], a[8:11], a[36:39], v[12:15]\n\t"
      "ds_read_b128 a[104:107], v36 offset:2880\n\t"
      "v_mfma_f32_16x16x32_bf16 v[12:15], a[12:15], a[32:35], v[12:15]\n\t"
      "ds_read2_b32 a[76:77], v33 offset0:208 offset1:209\n\t"
      "v_mfma_f32_16x16x32_bf16 v[16:19], a[0:3], a[48:51], v[16:19]\n\t"
      "ds_read2_b32 a[78:79], v33 offset0:210 offset1:211\n\t"
      "v_mfma_f32_16x16x32_bf16 v[16:19], a[4:7], a[44:47], v[16:19]\n\t"
      "ds_read_b128 a[108:111], v36 offset:2368\n\t"
      "v_mfma_f32_16x16x32_bf16 v[16:19], a[8:11], a[40:43], v[16:19]\n\t"
      "ds_read2_b32 a[80:81], v34 offset0:80 offset1:81\n\t"
      "v_mfma_f32_16x16x32_bf16 v[16:19], a[12:15], a[36:39], v[16:19]\n\t"
      "ds_read2_b32 a[82:83], v34 offset0:82 offset1:83\n\t"
      "v_mfma_f32_16x16x32_bf16 v[16:19], a[16:19], a[32:35], v[16:19]\n\t"
      "ds_read_b128 a[112:115], v36 offset:1856\n\t"
      "v_mfma_f32_16x16x32_bf16 v[20:23], a[0:3], a[52:55], v[20:23]\n\t"
      "ds_read2_b32 a[84:85], v34 offset0:208 offset1:209\n\t"
      "v_mfma_f32_16x16x32_bf16 v[20:23], a[4:7], a[48:51], v[20:23]\n\t"
      "ds_read2_b32 a[86:87], v34 offset0:210 offset1:211\n\t"
      "v_mfma_f32_16x16x32_bf16 v[20:23], a[8:11], a[44:47], v[20:23]\n\t"
      "ds_read_b128 a[116:119], v36 offset:1344\n\t"
      "v_mfma_f32_16x16x32_bf16 v[20:23], a[12:15], a[40:43], v[20:23]\n\t"
      "ds_read2_b32 a[88:89], v35 offset0:80 offset1:81\n\t"
      "v_mfma_f32_16x16x32_bf16 v[20:23], a[16:19], a[36:39], v[20:23]\n\t"
      "ds_read2_b32 a[90:91], v35 offset0:82 offset1:83\n\t"
      "v_mfma_f32_16x16x32_bf16 v[20:23], a[20:23], a[32:35], v[20:23]\n\t"
      "ds_read_b128 a[120:123], v36 offset:832\n\t"
      "v_mfma_f32_16x16x32_bf16 v[24:27], a[0:3], a[56:59], v[24:27]\n\t"
      "ds_read2_b32 a[92:93], v35 offset0:208 offset1:209\n\t"
      "v_mfma_f32_16x16x32_bf16 v[24:27], a[4:7], a[52:55], v[24:27]\n\t"
      "ds_read2_b32 a[94:95], v35 offset0:210 offset1:211\n\t"
      "v_mfma_f32_16x16x32_bf16 v[24:27], a[8:11], a[48:51], v[24:27]\n\t"
      "ds_read_b128 a[124:127], v36 offset:320\n\t"
      "v_mfma_f32_16x16x32_bf16 v[24:27], a[12:15], a[44:47], v[24:27]\n\t"
      "v_mfma_f32_16x16x32_bf16 v[24:27], a[16:19], a[40:43], v[24:27]\n\t"
      "v_mfma_f32_16x16x32_bf16 v[24:27], a[20:23], a[36:39], v[24:27]\n\t"
      "v_mfma_f32_16x16x32_bf16 v[24:27], a[24:27], a[32:35], v[24:27]\n\t"
      "v_mfma_f32_16x16x32_bf16 v[28:31], a[0:3], a[60:63], v[28:31]\n\t"
      "v_mfma_f32_16x16x32_bf16 v[28:31], a[4:7], a[56:59], v[28:31]\n\t"
      "v_mfma_f32_16x16x32_bf16 v[28:31], a[8:11], a[52:55], v[28:31]\n\t"
      "v_mfma_f32_16x16x32_bf16 v[28:31], a[12:15], a[48:51], v[28:31]\n\t"
      "v_mfma_f32_16x16x32_bf16 v[28:31], a[16:19], a[44:47], v[28:31]\n\t"
      "v_mfma_f32_16x16x32_bf16 v[28:31], a[20:23], a[40:43], v[28:31]\n\t"
      "v_mfma_f32_16x16x32_bf16 v[28:31], a[24:27], a[36:39], v[28:31]\n\t"
      "v_mfma_f32_16x16x32_bf16 v[28:31], a[28:31], a[32:35], v[28:31]\n\t"
      "s_waitcnt lgkmcnt(0)\n\t"
      "v_mfma_f32_16x16x32_bf16 v[0:3], a[64:67], a[96:99], v[0:3]\n\t"
      "ds_read2_b32 a[0:1], v32 offset0:96 offset1:97\n\t"
      "v_mfma_f32_16x16x32_bf16 v[4:7], a[64:67], a[100:103], v[4:7]\n\t"
      "ds_read2_b32 a[2:3], v32 offset0:98 offset1:99\n\t"
      "v_mfma_f32_16x16x32_bf16 v[4:7], a[68:71], a[96:99], v[4:7]\n\t"
      "ds_read_b128 a[32:35], v36 offset:3968\n\t"
      "v_mfma_f32_16x16x32_bf16 v[8:11], a[64:67], a[104:107], v[8:11]\n\t"
      "ds_read2_b32 a[4:5], v32 offset0:224 offset1:225\n\t"
      "v_mfma_f32_16x16x32_bf16 v[8:11], a[68:71], a[100:103], v[8:11]\n\t"
      "ds_read2_b32 a[6:7], v32 offset0:226 offset1:227\n\t"
      "v_mfma_f32_16x16x32_bf16 v[8:11], a[72:75], a[96:99], v[8:11]\n\t"
      "ds_read_b128 a[36:39], v36 offset:3456\n\t"
      "v_mfma_f32_16x16x32_bf16 v[12:15], a[64:67], a[108:111], v[12:15]\n\t"
      "ds_read2_b32 a[8:9], v33 offset0:96 offset1:97\n\t"
      "v_mfma_f32_16x16x32_bf16 v[12:15], a[68:71], a[104:107], v[12:15]\n\t"
      "ds_read2_b32 a[10:11], v33 offset0:98 offset1:99\n\t"
      "v_mfma_f32_16x16x32_bf16 v[12:15], a[72:75], a[100:103], v[12:15]\n\t"
      "ds_read_b128 a[40:43], v36 offset:2944\n\t"
      "v_mfma_f32_16x16x32_bf16 v[12:15], a[76:79], a[96:99], v[12:15]\n\t"
      "ds_read2_b32 a[12:13], v33 offset0:224 offset1:225\n\t"
      "v_mfma_f32_16x16x32_bf16 v[16:19], a[64:67], a[112:115], v[16:19]\n\t"
      "ds_read2_b32 a[14:15], v33 offset0:226 offset1:227\n\t"
      "v_mfma_f32_16x16x32_bf16 v[16:19], a[68:71], a[108:111], v[16:19]\n\t"
      "ds_read_b128 a[44:47], v36 offset:2432\n\t"
      "v_mfma_f32_16x16x32_bf16 v[16:19], a[72:75], a[104:107], v[16:19]\n\t"
      "ds_read2_b32 a[16:17], v34 offset0:96 offset1:97\n\t"
      "v_mfma_f32_16x16x32_bf16 v[16:19], a[76:79], a[100:103], v[16:19]\n\t"
      "ds_read2_b32 a[18:19], v34 offset0:98 offset1:99\n\t"
      "v_mfma_f32_16x16x32_bf16 v[16:19], a[80:83], a[96:99], v[16:19]\n\t"
      "ds_read_b128 a[48:51], v36 offset:1920\n\t"
      "v_mfma_f32_16x16x32_bf16 v[20:23], a[64:67], a[116:119], v[20:23]\n\t"
      "ds_read2_b32 a[20:21], v34 offset0:224 offset1:225\n\t"
      "v_mfma_f32_16x16x32_bf16 v[20:23], a[68:71], a[112:115], v[20:23]\n\t"
      "ds_read2_b32 a[22:23], v34 offset0:226 offset1:227\n\t"
      "v_mfma_f32_16x16x32_bf16 v[20:23], a[72:75], a[108:111], v[20:23]\n\t"
      "ds_read_b128 a[52:55], v36 offset:1408\n\t"
      "v_mfma_f32_16x16x32_bf16 v[20:23], a[76:79], a[104:107], v[20:23]\n\t"
      "ds_read2_b32 a[24:25], v35 offset0:96 offset1:97\n\t"
      "v_mfma_f32_16x16x32_bf16 v[20:23], a[80:83], a[100:103], v[20:23]\n\t"
      "ds_read2_b32 a[26:27], v35 offset0:98 offset1:99\n\t"
      "v_mfma_f32_16x16x32_bf16 v[20:23], a[84:87], a[96:99], v[20:23]\n\t"
      "ds_read_b128 a[56:59], v36 offset:896\n\t"
      "v_mfma_f32_16x16x32_bf16 v[24:27], a[64:67], a[120:123], v[24:27]\n\t"
      "ds_read2_b32 a[28:29], v35 offset0:224 offset1:225\n\t"
      "v_mfma_f32_16x16x32_bf16 v[24:27], a[68:71], a[116:119], v[24:27]\n\t"
      "ds_read2_b32 a[30:31], v35 offset0:226 offset1:227\n\t"
      "v_mfma_f32_16x16x32_bf16 v[24:27], a[72:75], a[112:115], v[24:27]\n\t"
      "ds_read_b128 a[60:63], v36 offset:384\n\t"
      "v_mfma_f32_16x16x32_bf16 v[24:27], a[76:79], a[108:111], v[24:27]\n\t"
      "v_mfma_f32_16x16x32_bf16 v[24:27], a[80:83], a[104:107], v[24:27]\n\t"
      "v_mfma_f32_16x16x32_bf16 v[24:27], a[84:87], a[100:103], v[24:27]\n\t"
      "v_mfma_f32_16x16x32_bf16 v[24:27], a[88:91], a[96:99], v[24:27]\n\t"
      "v_mfma_f32_16x16x32_bf16 v[28:31], a[64:67], a[124:127], v[28:31]\n\t"
      "v_mfma_f32_16x16x32_bf16 v[28:31], a[68:71], a[120:123], v[28:31]\n\t"
      "v_mfma_f32_16x16x32_bf16 v[28:31], a[72:75], a[116:119], v[28:31]\n\t"
      "v_mfma_f32_16x16x32_bf16 v[28:31], a[76:79], a[112:115], v[28:31]\n\t"
      "v_mfma_f32_16x16x32_bf16 v[28:31], a[80:83], a[108:111], v[28:31]\n\t"
      "v_mfma_f32_16x16x32_bf16 v[28:31], a[84:87], a[104:107], v[28:31]\n\t"
      "v_mfma_f32_16x16x32_bf16 v[28:31], a[88:91], a[100:103], v[28:31]\n\t"
      "v_mfma_f32_16x16x32_bf16 v[28:31], a[92:95], a[96:99], v[28:31]\n\t"
      "s_waitcnt lgkmcnt(0)\n\t"
      "v_mfma_f32_16x16x32_bf16 v[0:3], a[0:3], a[32:35], v[0:3]\n\t"
      "ds_read2_b32 a[64:65], v32 offset0:112 offset1:113\n\t"
      "v_mfma_f32_16x16x32_bf16 v[4:7], a[0:3], a[36:39], v[4:7]\n\t"
      "ds_read2_b32 a[66:67], v32 offset0:114 offset1:115\n\t"
      "v_mfma_f32_16x16x32_bf16 v[4:7], a[4:7], a[32:35], v[4:7]\n\t"
      "ds_read_b128 a[96:99], v36 offset:4032\n\t"
      "v_mfma_f32_16x16x32_bf16 v[8:11], a[0:3], a[40:43], v[8:11]\n\t"
      "ds_read2_b32 a[68:69], v32 offset0:240 offset1:241\n\t"
      "v_mfma_f32_16x16x32_bf16 v[8:11], a[4:7], a[36:39], v[8:11]\n\t"
      "ds_read2_b32 a[70:71], v32 offset0:242 offset1:243\n\t"
      "v_mfma_f32_16x16x32_bf16 v[8:11], a[8:11], a[32:35], v[8:11]\n\t"
      "ds_read_b128 a[100:103], v36 offset:3520\n\t"
      "v_mfma_f32_16x16x32_bf16 v[12:15], a[0:3], a[44:47], v[12:15]\n\t"
      "ds_read2_b32 a[72:73], v33 offset0:112 offset1:113\n\t"
      "v_mfma_f32_16x16x32_bf16 v[12:15], a[4:7], a[40:43], v[12:15]\n\t"
      "ds_read2_b32 a[74:75], v33 offset0:114 offset1:115\n\t"
      "v_mfma_f32_16x16x32_bf16 v[12:15], a[8:11], a[36:39], v[12:15]\n\t"
      "ds_read_b128 a[104:107], v36 offset:3008\n\t"
      "v_mfma_f32_16x16x32_bf16 v[12:15], a[12:15], a[32:35], v[12:15]\n\t"
      "ds_read2_b32 a[76:77], v33 offset0:240 offset1:241\n\t"
      "v_mfma_f32_16x16x32_bf16 v[16:19], a[0:3], a[48:51], v[16:19]\n\t"
      "ds_read2_b32 a[78:79], v33 offset0:242 offset1:243\n\t"
      "v_mfma_f32_16x16x32_bf16 v[16:19], a[4:7], a[44:47], v[16:19]\n\t"
      "ds_read_b128 a[108:111], v36 offset:2496\n\t"
      "v_mfma_f32_16x16x32_bf16 v[16:19], a[8:11], a[40:43], v[16:19]\n\t"
      "ds_read2_b32 a[80:81], v34 offset0:112 offset1:113\n\t"
      "v_mfma_f32_16x16x32_bf16 v[16:19], a[12:15], a[36:39], v[16:19]\n\t"
      "ds_read2_b32 a[82:83], v34 offset0:114 offset1:115\n\t"
      "v_mfma_f32_16x16x32_bf16 v[16:19], a[16:19], a[32:35], v[16:19]\n\t"
      "ds_read_b128 a[112:115], v36 offset:1984\n\t"
      "v_mfma_f32_16x16x32_bf16 v[20:23], a[0:3], a[52:55], v[20:23]\n\t"
      "ds_read2_b32 a[84:85], v34 offset0:240 offset1:241\n\t"
      "v_mfma_f32_16x16x32_bf16 v[20:23], a[4:7], a[48:51], v[20:23]\n\t"
      "ds_read2_b32 a[86:87], v34 offset0:242 offset1:243\n\t"
      "v_mfma_f32_16x16x32_bf16 v[20:23], a[8:11], a[44:47], v[20:23]\n\t"
      "ds_read_b128 a[116:119], v36 offset:1472\n\t"
      "v_mfma_f32_16x16x32_bf16 v[20:23], a[12:15], a[40:43], v[20:23]\n\t"
      "ds_read2_b32 a[88:89], v35 offset0:112 offset1:113\n\t"
      "v_mfma_f32_16x16x32_bf16 v[20:23], a[16:19], a[36:39], v[20:23]\n\t"
      "ds_read2_b32 a[90:91], v35 offset0:114 offset1:115\n\t"
      "v_mfma_f32_16x16x32_bf16 v[20:23], a[20:23], a[32:35], v[20:23]\n\t"
      "ds_read_b128 a[120:123], v36 offset:960\n\t"
      "v_mfma_f32_16x16x32_bf16 v[24:27], a[0:3], a[56:59], v[24:27]\n\t"
      "ds_read2_b32 a[92:93], v35 offset0:240 offset1:241\n\t"
      "v_mfma_f32_16x16x32_bf16 v[24:27], a[4:7], a[52:55], v[24:27]\n\t"
      "ds_read2_b32 a[94:95], v35 offset0:242 offset1:243\n\t"
      "v_mfma_f32_16x16x32_bf16 v[24:27], a[8:11], a[48:51], v[24:27]\n\t"
      "ds_read_b128 a[124:127], v36 offset:448\n\t"
      "v_mfma_f32_16x16x32_bf16 v[24:27], a[12:15], a[44:47], v[24:27]\n\t"
      "v_mfma_f32_16x16x32_bf16 v[24:27], a[16:19], a[40:43], v[24:27]\n\t"
      "v_mfma_f32_16x16x32_bf16 v[24:27], a[20:23], a[36:39], v[24:27]\n\t"
      "v_mfma_f32_16x16x32_bf16 v[24:27], a[24:27], a[32:35], v[24:27]\n\t"
      "v_mfma_f32_16x16x32_bf16 v[28:31], a[0:3], a[60:63], v[28:31]\n\t"
      "v_mfma_f32_16x16x32_bf16 v[28:31], a[4:7], a[56:59], v[28:31]\n\t"
      "v_mfma_f32_16x16x32_bf16 v[28:31], a[8:11], a[52:55], v[28:31]\n\t"
      "v_mfma_f32_16x16x32_bf16 v[28:31], a[12:15], a[48:51], v[28:31]\n\t"
      "v_mfma_f32_16x16x32_bf16 v[28:31], a[16:19], a[44:47], v[28:31]\n\t"
      "v_mfma_f32_16x16x32_bf16 v[28:31], a[20:23], a[40:43], v[28:31]\n\t"
      "v_mfma_f32_16x16x32_bf16 v[28:31], a[24:27], a[36:39], v[28:31]\n\t"
      "v_mfma_f32_16x16x32_bf16 v[28:31], a[28:31], a[32:35], v[28:31]\n\t"
      "s_waitcnt lgkmcnt(0)\n\t"
      "v_mfma_f32_16x16x32_bf16 v[0:3], a[64:67], a[96:99], v[0:3]\n\t"
      "v_mfma_f32_16x16x32_bf16 v[4:7], a[64:67], a[100:103], v[4:7]\n\t"
      "v_mfma_f32_16x16x32_bf16 v[4:7], a[68:71], a[96:99], v[4:7]\n\t"
      "v_mfma_f32_16x16x32_bf16 v[8:11], a[64:67], a[104:107], v[8:11]\n\t"
      "v_mfma_f32_16x16x32_bf16 v[8:11], a[68:71], a[100:103], v[8:11]\n\t"
      "v_mfma_f32_16x16x32_bf16 v[8:11], a[72:75], a[96:99], v[8:11]\n\t"
      "v_mfma_f32_16x16x32_bf16 v[12:15], a[64:67], a[108:111], v[12:15]\n\t"
      "v_mfma_f32_16x16x32_bf16 v[12:15], a[68:71], a[104:107], v[12:15]\n\t"
      "v_mfma_f32_16x16x32_bf16 v[12:15], a[72:75], a[100:103], v[12:15]\n\t"
      "v_mfma_f32_16x16x32_bf16 v[12:15], a[76:79], a[96:99], v[12:15]\n\t"
      "v_mfma_f32_16x16x32_bf16 v[16:19], a[64:67], a[112:115], v[16:19]\n\t"
      "v_mfma_f32_16x16x32_bf16 v[16:19], a[68:71], a[108:111], v[16:19]\n\t"
      "v_mfma_f32_16x16x32_bf16 v[16:19], a[72:75], a[104:107], v[16:19]\n\t"
      "v_mfma_f32_16x16x32_bf16 v[16:19], a[76:79], a[100:103], v[16:19]\n\t"
      "v_mfma_f32_16x16x32_bf16 v[16:19], a[80:83], a[96:99], v[16:19]\n\t"
      "v_mfma_f32_16x16x32_bf16 v[20:23], a[64:67], a[116:119], v[20:23]\n\t"
      "v_mfma_f32_16x16x32_bf16 v[20:23], a[68:71], a[112:115], v[20:23]\n\t"
      "v_mfma_f32_16x16x32_bf16 v[20:23], a[72:75], a[108:111], v[20:23]\n\t"
      "v_mfma_f32_16x16x32_bf16 v[20:23], a[76:79], a[104:107], v[20:23]\n\t"
      "v_mfma_f32_16x16x32_bf16 v[20:23], a[80:83], a[100:103], v[20:23]\n\t"
      "v_mfma_f32_16x16x32_bf16 v[20:23], a[84:87], a[96:99], v[20:23]\n\t"
      "v_mfma_f32_16x16x32_bf16 v[24:27], a[64:67], a[120:123], v[24:27]\n\t"
      "v_mfma_f32_16x16x32_bf16 v[24:27], a[68:71], a[116:119], v[24:27]\n\t"
      "v_mfma_f32_16x16x32_bf16 v[24:27], a[72:75], a[112:115], v[24:27]\n\t"
      "v_mfma_f32_16x16x32_bf16 v[24:27], a[76:79], a[108:111], v[24:27]\n\t"
      "v_mfma_f32_16x16x32_bf16 v[24:27], a[80:83], a[104:107], v[24:27]\n\t"
      "v_mfma_f32_16x16x32_bf16 v[24:27], a[84:87], a[100:103], v[24:27]\n\t"
      "v_mfma_f32_16x16x32_bf16 v[24:27], a[88:91], a[96:99], v[24:27]\n\t"
      "v_mfma_f32_16x16x32_bf16 v[28:31], a[64:67], a[124:127], v[28:31]\n\t"
      "v_mfma_f32_16x16x32_bf16 v[28:31], a[68:71], a[120:123], v[28:31]\n\t"
      "v_mfma_f32_16x16x32_bf16 v[28:31], a[72:75], a[116:119], v[28:31]\n\t"
      "v_mfma_f32_16x16x32_bf16 v[28:31], a[76:79], a[112:115], v[28:31]\n\t"
      "v_mfma_f32_16x16x32_bf16 v[28:31], a[80:83], a[108:111], v[28:31]\n\t"
      "v_mfma_f32_16x16x32_bf16 v[28:31], a[84:87], a[104:107], v[28:31]\n\t"
      "v_mfma_f32_16x16x32_bf16 v[28:31], a[88:91], a[100:103], v[28:31]\n\t"
      "v_mfma_f32_16x16x32_bf16 v[28:31], a[92:95], a[96:99], v[28:31]\n\t"
      "s_nop 7\n\t"
      "s_nop 7\n\t"
          : "={v[0:31]}"(acc) : "{v[32:39]}"(ad) : "memory", "a0", "a1", "a2", "a3", "a4", "a5", "a6", "a7", "a8", "a9", "a10", "a11", "a12", "a13", "a14", "a15", "a16", "a17", "a18", "a19", "a20", "a21", "a22", "a23", "a24", "a25", "a26", "a27", "a28", "a29", "a30", "a31", "a32", "a33", "a34", "a35", "a36", "a37", "a38", "a39", "a40", "a41", "a42", "a43", "a44", "a45", "a46", "a47", "a48", "a49", "a50", "a51", "a52", "a53", "a54", "a55", "a56", "a57", "a58", "a59", "a60", "a61", "a62", "a63", "a64", "a65", "a66", "a67", "a68", "a69", "a70", "a71", "a72", "a73", "a74", "a75", "a76", "a77", "a78", "a79", "a80", "a81", "a82", "a83", "a84", "a85", "a86", "a87", "a88", "a89", "a90", "a91", "a92", "a93", "a94", "a95", "a96", "a97", "a98", "a99", "a100", "a101", "a102", "a103", "a104", "a105", "a106", "a107", "a108", "a109", "a110", "a111", "a112", "a113", "a114", "a115", "a116", "a117", "a118", "a119", "a120", "a121", "a122", "a123", "a124", "a125", "a126", "a127");
    }
#pragma unroll
    for (int it = 0; it < 8; ++it) {
      const int t = 256 * it + 16 * j + 4 * q;
      const u32x2 kv = *(const LAS u32x2*)(wb + XOFF + 2 * (2044 - t));
      const float y0 = fmaf(hi16(kv[1]), dd, acc[4 * it]), y1 = fmaf(lo16(kv[1]), dd, acc[4 * it + 1]), y2 = fmaf(hi16(kv[0]), dd, acc[4 * it + 2]), y3 = fmaf(lo16(kv[0]), dd, acc[4 * it + 3]);
      *(LAS u32x2*)(wb + 2 * t) = u32x2{pk2(y0, y1), pk2(y2, y3)};
    }
    asm volatile("s_waitcnt lgkmcnt(0)" ::: "memory");
    BARRIER();
    {
      const int g = lane >> 4, qq = (lane >> 2) & 3, p = lane & 3, odd = lane & 1;
#pragma unroll
      for (int i = 0; i < 8; i += 2) {
        const int T = wid * 512 + i * 64 + g * 16;
        const u32x2 a = __builtin_bit_cast(u32x2, __builtin_amdgcn_ds_read_tr16_b64_v4i16((LAS s16x4*)((LAS char*)smem + qq * WSTR + 2 * (T + 4 * p))));
        const u32x2 b = __builtin_bit_cast(u32x2, __builtin_amdgcn_ds_read_tr16_b64_v4i16((LAS s16x4*)((LAS char*)smem + qq * WSTR + 2 * (T + 64 + 4 * p))));
        const u32 s0 = odd ? a[0] : b[0], s1 = odd ? a[1] : b[1];
        const u32 r0_ = __builtin_amdgcn_update_dpp(0, s0, 0xB1, 0xf, 0xf, true), r1_ = __builtin_amdgcn_update_dpp(0, s1, 0xB1, 0xf, 0xf, true);
        const u32x4 w = odd ? u32x4{r0_, r1_, b[0], b[1]} : u32x4{a[0], a[1], r0_, r1_};
        const int t = T + (lane & 14) + (odd ? 64 : 0);
        st16_dev((char*)yb + (size_t)(t >> 3) * YBS + bid * 64 + (t & 7) * 8, w);
      }
    }
    asm volatile("s_waitcnt vmcnt(0)" : "+v"(xv[0][0]), "+v"(xv[0][1]), "+v"(xv[1][0]), "+v"(xv[1][1]) : "v"(pfd) : "memory");
    BARRIER();
    if (wid == 0 && lane < 8) signal_flag(flagA + lane * 256 + bid, tag);
  }

  {
    if (wid == 0) {
      poll_flags256(flagA + (bid & 7) * 256, tag, lane);
      if (tid == 0 && bid == 0) signal_flag(fl, tag);
    }
    BARRIER();
    {
      const auto srdY = mk_rsrc((const char*)yb + (size_t)bid * YBS, 16384);
      const int yo = wid * 4096 + (lane >> 2) * 64 + (((lane & 3) ^ ((lane >> 4) & 3)) * 16);
      asm volatile("s_nop 4\n\ts_mov_b32 m0, %[lds]\n\ts_nop 0\n\tbuffer_load_dwordx4 %[v0], %[srd], 0 offen sc1 lds\n\t"
                   "s_add_u32 m0, %[lds], 1024\n\ts_nop 0\n\tbuffer_load_dwordx4 %[v0], %[srd], %[s1] offen sc1 lds\n\t"
                   "s_add_u32 m0, %[lds], 2048\n\ts_nop 0\n\tbuffer_load_dwordx4 %[v0], %[srd], %[s2] offen sc1 lds\n\t"
                   "s_add_u32 m0, %[lds], 3072\n\ts_nop 0\n\tbuffer_load_dwordx4 %[v0], %[srd], %[s3] offen sc1 lds\n\t"
                   "s_waitcnt vmcnt(0)"
                   :: [lds] "s"(sbase + wid * 4096), [v0] "v"(yo), [srd] "s"(srdY), [s1] "s"(1024), [s2] "s"(2048), [s3] "s"(3072) : "memory", "scc");
    }
    BARRIER();
    u32x4 yv[2][2];
#pragma unroll
    for (int i = 0; i < 2; ++i)
#pragma unroll
      for (int k = 0; k < 2; ++k) yv[i][k] = *(const LAS u32x4*)((LAS char*)smem + (2 * (lane + 64 * i) + k) * 64 + ((wid ^ ((lane >> 1) & 3)) * 16));
    f32x2 xf[2][8], hf[2][8];
    u32x4 ho[2][2], no[2][2];
    float s[2], mean[2], rs[2];
#pragma unroll
    for (int a = 0; a < 2; ++a) {
      f32x2 t = {0.f, 0.f};
#pragma unroll
      for (int e = 0; e < 8; ++e) { const u32 w = xv[a][e >> 2][e & 3]; xf[a][e] = f32x2{lo16(w), hi16(w)}; t += xf[a][e]; }
      s[a] = t.x + t.y;
    }
#pragma unroll
    for (int a = 0; a < 2; ++a) mean[a] = wave_sum(s[a]) * (1.f / D_);
#pragma unroll
    for (int a = 0; a < 2; ++a) {
      f32x2 t = {0.f, 0.f};
#pragma unroll
      for (int e = 0; e < 8; ++e) { const f32x2 d = xf[a][e] - mean[a]; t += d * d; }
      s[a] = t.x + t.y;
    }
#pragma unroll
    for (int a = 0; a < 2; ++a) { const float var = wave_sum(s[a]) * (1.f / D_); rs[a] = __builtin_amdgcn_rcpf(rb(__builtin_amdgcn_sqrtf(rb(rb(var) + 1e-5f)))); mean[a] = rb(mean[a]); }
#pragma unroll
    for (int a = 0; a < 2; ++a) {
      f32x2 t = {0.f, 0.f};
#pragma unroll
      for (int e = 0; e < 8; ++e) {
        const u32 y = yv[e >> 2][(e >> 1) & 1][2 * a + (e & 1)];
        const f32x2 gq = ((xf[a][e] - mean[a]) * rs[a]) * f32x2{lo16(y), hi16(y)};
        const f32x2 hv2 = xf[a][e] + gq;
        const u32 hh = pk2(hv2.x, hv2.y);
        ho[a][e >> 2][e & 3] = hh; hf[a][e] = f32x2{lo16(hh), hi16(hh)}; t += hf[a][e];
      }
      s[a] = t.x + t.y;
    }
#pragma unroll
    for (int a = 0; a < 2; ++a) mean[a] = wave_sum(s[a]) * (1.f / D_);
#pragma unroll
    for (int a = 0; a < 2; ++a) {
      f32x2 t = {0.f, 0.f};
#pragma unroll
      for (int e = 0; e < 8; ++e) { const f32x2 d = hf[a][e] - mean[a]; t += d * d; }
      s[a] = t.x + t.y;
    }
#pragma unroll
    for (int a = 0; a < 2; ++a) { const float var = wave_sum(s[a]) * (1.f / D_); rs[a] = __builtin_amdgcn_rcpf(rb(__builtin_amdgcn_sqrtf(rb(rb(var) + 1e-5f)))); mean[a] = rb(mean[a]); }
#pragma unroll
    for (int a = 0; a < 2; ++a)
#pragma unroll
      for (int e = 0; e < 8; ++e) { const f32x2 n = (hf[a][e] - mean[a]) * rs[a]; no[a][e >> 2][e & 3] = pk2(n.x, n.y); }
#pragma unroll
    for (int a = 0; a < 2; ++a)
#pragma unroll
      for (int i = 0; i < 2; ++i) {
        const size_t o = ((size_t)bid * 8 + 2 * wid + a) * D_ + 8 * (lane + 64 * i);
        st16_dev(hn + o, no[a][i]); st16_dev(h + o, ho[a][i]);
      }
    DRAIN();
    BARRIER();
    if (tid == 0) signal_flag(flagB + bid, tag);
  }

  {
    f32x32 A0, A1, A2, A3, A4, A5, A6;
    int ln = lane; asm volatile("" : "+v"(ln));
    const int wm = wid >> 1, wn = wid & 1, c = ln & 15, q = ln >> 4;
    asm volatile(
      "v_mov_b32 v0, 0\n\t"
      "v_mov_b32 v1, 0\n\t"
      "v_mov_b32 v2, 0\n\t"
      "v_mov_b32 v3, 0\n\t"
      "v_mov_b32 v4, 0\n\t"
      "v_mov_b32 v5, 0\n\t"
      "v_mov_b32 v6, 0\n\t"
      "v_mov_b32 v7, 0\n\t"
      "v_mov_b32 v8, 0\n\t"
      "v_mov_b32 v9, 0\n\t"
      "v_mov_b32 v10, 0\n\t"
      "v_mov_b32 v11, 0\n\t"
      "v_mov_b32 v12, 0\n\t"
      "v_mov_b32 v13, 0\n\t"
      "v_mov_b32 v14, 0\n\t"
      "v_mov_b32 v15, 0\n\t"
      "v_mov_b32 v16, 0\n\t"
      "v_mov_b32 v17, 0\n\t"
      "v_mov_b32 v18, 0\n\t"
      "v_mov_b32 v19, 0\n\t"
      "v_mov_b32 v20, 0\n\t"
      "v_mov_b32 v21, 0\n\t"
      "v_mov_b32 v22, 0\n\t"
      "v_mov_b32 v23, 0\n\t"
      "v_mov_b32 v24, 0\n\t"
      "v_mov_b32 v25, 0\n\t"
      "v_mov_b32 v26, 0\n\t"
      "v_mov_b32 v27, 0\n\t"
      "v_mov_b32 v28, 0\n\t"
      "v_mov_b32 v29, 0\n\t"
      "v_mov_b32 v30, 0\n\t"
      "v_mov_b32 v31, 0\n\t"
      "v_mov_b32 v32, 0\n\t"
      "v_mov_b32 v33, 0\n\t"
      "v_mov_b32 v34, 0\n\t"
      "v_mov_b32 v35, 0\n\t"
      "v_mov_b32 v36, 0\n\t"
      "v_mov_b32 v37, 0\n\t"
      "v_mov_b32 v38, 0\n\t"
      "v_mov_b32 v39, 0\n\t"
      "v_mov_b32 v40, 0\n\t"
      "v_mov_b32 v41, 0\n\t"
      "v_mov_b32 v42, 0\n\t"
      "v_mov_b32 v43, 0\n\t"
      "v_mov_b32 v44, 0\n\t"
      "v_mov_b32 v45, 0\n\t"
      "v_mov_b32 v46, 0\n\t"
      "v_mov_b32 v47, 0\n\t"
      "v_mov_b32 v48, 0\n\t"
      "v_mov_b32 v49, 0\n\t"
      "v_mov_b32 v50, 0\n\t"
      "v_mov_b32 v51, 0\n\t"
      "v_mov_b32 v52, 0\n\t"
      "v_mov_b32 v53, 0\n\t"
      "v_mov_b32 v54, 0\n\t"
      "v_mov_b32 v55, 0\n\t"
      "v_mov_b32 v56, 0\n\t"
      "v_mov_b32 v57, 0\n\t"
      "v_mov_b32 v58, 0\n\t"
      "v_mov_b32 v59, 0\n\t"
      "v_mov_b32 v60, 0\n\t"
      "v_mov_b32 v61, 0\n\t"
      "v_mov_b32 v62, 0\n\t"
      "v_mov_b32 v63, 0\n\t"
      "v_mov_b32 v64, 0\n\t"
      "v_mov_b32 v65, 0\n\t"
      "v_mov_b32 v66, 0\n\t"
      "v_mov_b32 v67, 0\n\t"
      "v_mov_b32 v68, 0\n\t"
      "v_mov_b32 v69, 0\n\t"
      "v_mov_b32 v70, 0\n\t"
      "v_mov_b32 v71, 0\n\t"
      "v_mov_b32 v72, 0\n\t"
      "v_mov_b32 v73, 0\n\t"
      "v_mov_b32 v74, 0\n\t"
      "v_mov_b32 v75, 0\n\t"
      "v_mov_b32 v76, 0\n\t"
      "v_mov_b32 v77, 0\n\t"
      "v_mov_b32 v78, 0\n\t"
      "v_mov_b32 v79, 0\n\t"
      "v_mov_b32 v80, 0\n\t"
      "v_mov_b32 v81, 0\n\t"
      "v_mov_b32 v82, 0\n\t"
      "v_mov_b32 v83, 0\n\t"
      "v_mov_b32 v84, 0\n\t"
      "v_mov_b32 v85, 0\n\t"
      "v_mov_b32 v86, 0\n\t"
      "v_mov_b32 v87, 0\n\t"
      "v_mov_b32 v88, 0\n\t"
      "v_mov_b32 v89, 0\n\t"
      "v_mov_b32 v90, 0\n\t"
      "v_mov_b32 v91, 0\n\t"
      "v_mov_b32 v92, 0\n\t"
      "v_mov_b32 v93, 0\n\t"
      "v_mov_b32 v94, 0\n\t"
      "v_mov_b32 v95, 0\n\t"
      "v_mov_b32 v96, 0\n\t"
      "v_mov_b32 v97, 0\n\t"
      "v_mov_b32 v98, 0\n\t"
      "v_mov_b32 v99, 0\n\t"
      "v_mov_b32 v100, 0\n\t"
      "v_mov_b32 v101, 0\n\t"
      "v_mov_b32 v102, 0\n\t"
      "v_mov_b32 v103, 0\n\t"
      "v_mov_b32 v104, 0\n\t"
      "v_mov_b32 v105, 0\n\t"
      "v_mov_b32 v106, 0\n\t"
      "v_mov_b32 v107, 0\n\t"
      "v_mov_b32 v108, 0\n\t"
      "v_mov_b32 v109, 0\n\t"
      "v_mov_b32 v110, 0\n\t"
      "v_mov_b32 v111, 0\n\t"
      "v_mov_b32 v112, 0\n\t"
      "v_mov_b32 v113, 0\n\t"
      "v_mov_b32 v114, 0\n\t"
      "v_mov_b32 v115, 0\n\t"
      "v_mov_b32 v116, 0\n\t"
      "v_mov_b32 v117, 0\n\t"
      "v_mov_b32 v118, 0\n\t"
      "v_mov_b32 v119, 0\n\t"
      "v_mov_b32 v120, 0\n\t"
      "v_mov_b32 v121, 0\n\t"
      "v_mov_b32 v122, 0\n\t"
      "v_mov_b32 v123, 0\n\t"
      "v_mov_b32 v124, 0\n\t"
      "v_mov_b32 v125, 0\n\t"
      "v_mov_b32 v126, 0\n\t"
      "v_mov_b32 v127, 0\n\t"
      "v_mov_b32 v128, 0\n\t"
      "v_mov_b32 v129, 0\n\t"
      "v_mov_b32 v130, 0\n\t"
      "v_mov_b32 v131, 0\n\t"
      "v_mov_b32 v132, 0\n\t"
      "v_mov_b32 v133, 0\n\t"
      "v_mov_b32 v134, 0\n\t"
      "v_mov_b32 v135, 0\n\t"
      "v_mov_b32 v136, 0\n\t"
      "v_mov_b32 v137, 0\n\t"
      "v_mov_b32 v138, 0\n\t"
      "v_mov_b32 v139, 0\n\t"
      "v_mov_b32 v140, 0\n\t"
      "v_mov_b32 v141, 0\n\t"
      "v_mov_b32 v142, 0\n\t"
      "v_mov_b32 v143, 0\n\t"
      "v_mov_b32 v144, 0\n\t"
      "v_mov_b32 v145, 0\n\t"
      "v_mov_b32 v146, 0\n\t"
      "v_mov_b32 v147, 0\n\t"
      "v_mov_b32 v148, 0\n\t"
      "v_mov_b32 v149, 0\n\t"
      "v_mov_b32 v150, 0\n\t"
      "v_mov_b32 v151, 0\n\t"
      "v_mov_b32 v152, 0\n\t"
      "v_mov_b32 v153, 0\n\t"
      "v_mov_b32 v154, 0\n\t"
      "v_mov_b32 v155, 0\n\t"
      "v_mov_b32 v156, 0\n\t"
      "v_mov_b32 v157, 0\n\t"
      "v_mov_b32 v158, 0\n\t"
      "v_mov_b32 v159, 0\n\t"
      "v_mov_b32 v160, 0\n\t"
      "v_mov_b32 v161, 0\n\t"
      "v_mov_b32 v162, 0\n\t"
      "v_mov_b32 v163, 0\n\t"
      "v_mov_b32 v164, 0\n\t"
      "v_mov_b32 v165, 0\n\t"
      "v_mov_b32 v166, 0\n\t"
      "v_mov_b32 v167, 0\n\t"
      "v_mov_b32 v168, 0\n\t"
      "v_mov_b32 v169, 0\n\t"
      "v_mov_b32 v170, 0\n\t"
      "v_mov_b32 v171, 0\n\t"
      "v_mov_b32 v172, 0\n\t"
      "v_mov_b32 v173, 0\n\t"
      "v_mov_b32 v174, 0\n\t"
      "v_mov_b32 v175, 0\n\t"
      "v_mov_b32 v176, 0\n\t"
      "v_mov_b32 v177, 0\n\t"
      "v_mov_b32 v178, 0\n\t"
      "v_mov_b32 v179, 0\n\t"
      "v_mov_b32 v180, 0\n\t"
      "v_mov_b32 v181, 0\n\t"
      "v_mov_b32 v182, 0\n\t"
      "v_mov_b32 v183, 0\n\t"
      "v_mov_b32 v184, 0\n\t"
      "v_mov_b32 v185, 0\n\t"
      "v_mov_b32 v186, 0\n\t"
      "v_mov_b32 v187, 0\n\t"
      "v_mov_b32 v188, 0\n\t"
      "v_mov_b32 v189, 0\n\t"
      "v_mov_b32 v190, 0\n\t"
      "v_mov_b32 v191, 0\n\t"
      "v_mov_b32 v192, 0\n\t"
      "v_mov_b32 v193, 0\n\t"
      "v_mov_b32 v194, 0\n\t"
      "v_mov_b32 v195, 0\n\t"
      "v_mov_b32 v196, 0\n\t"
      "v_mov_b32 v197, 0\n\t"
      "v_mov_b32 v198, 0\n\t"
      "v_mov_b32 v199, 0\n\t"
      "v_mov_b32 v200, 0\n\t"
      "v_mov_b32 v201, 0\n\t"
      "v_mov_b32 v202, 0\n\t"
      "v_mov_b32 v203, 0\n\t"
      "v_mov_b32 v204, 0\n\t"
      "v_mov_b32 v205, 0\n\t"
      "v_mov_b32 v206, 0\n\t"
      "v_mov_b32 v207, 0\n\t"
      "v_mov_b32 v208, 0\n\t"
      "v_mov_b32 v209, 0\n\t"
      "v_mov_b32 v210, 0\n\t"
      "v_mov_b32 v211, 0\n\t"
      "v_mov_b32 v212, 0\n\t"
      "v_mov_b32 v213, 0\n\t"
      "v_mov_b32 v214, 0\n\t"
      "v_mov_b32 v215, 0\n\t"
      "v_mov_b32 v216, 0\n\t"
      "v_mov_b32 v217, 0\n\t"
      "v_mov_b32 v218, 0\n\t"
      "v_mov_b32 v219, 0\n\t"
      "v_mov_b32 v220, 0\n\t"
      "v_mov_b32 v221, 0\n\t"
      "v_mov_b32 v222, 0\n\t"
      "v_mov_b32 v223, 0\n\t"
        : "={v[0:31]}"(A0), "={v[32:63]}"(A1), "={v[64:95]}"(A2), "={v[96:127]}"(A3), "={v[128:159]}"(A4), "={v[160:191]}"(A5), "={v[192:223]}"(A6));
    u32x8 vd;
    if (wid < 2) {
      const int r8 = ln >> 3, row0 = 128 * wid + r8;
#pragma unroll
      for (int j = 0; j < 8; ++j) vd[j] = (row0 + 8 * j) * 2048 + (((ln & 7) ^ (((r8 >> 1) + 4 * j) & 7)) * 16) - (j & 3) * 1024 + 4096;
    } else {
      const int r0_ = (ln * 4682) >> 16, p0_ = ln - 14 * r0_;
#pragma unroll
      for (int j = 0; j < 7; ++j) {
        const int ps = p0_ + (8 * j) % 14, cy = ps >= 14, pos = ps - 14 * cy, row = r0_ + 4 * j + (8 * j) / 14 + cy;
        vd[j] = row * 7168 + ((pos ^ ((row >> 3) & 1)) * 16) - (j & 3) * 1024 + 4096;
      }
      vd[7] = 0;
    }
    const auto srd = wid < 2 ? mk_rsrc((const char*)hn + (size_t)tok0 * 2048 - 4096, 256 * 2048 + 4096)
                             : mk_rsrc((const char*)(wid == 3 ? wu : wg) + f0 * 2 - 4096, 1024 * 7168 - f0 * 2 + 4096);
    const u32 ldsd = wid < 2 ? sbase + 16 * wid * 1024 : sbase + G1_WBASE + (wid == 3 ? G1_UOFF : 0);
    const int g = q, qr = (ln >> 2) & 3, p = ln & 3;
    u32x8 r0;
    r0[0] = sbase + (wm * 128 + c) * 128 + ((q ^ ((c >> 1) & 7)) * 16);
    r0[1] = sbase + (wm * 128 + c) * 128 + (((4 + q) ^ ((c >> 1) & 7)) * 16);
    const u32 wbase = sbase + G1_WBASE + (p < 2 ? 0 : G1_UOFF) + (8 * g + qr) * 224;
    r0[2] = wbase + (((wn * 7 + (p & 1)) ^ (g & 1)) * 16) + (p & 1) * 8;
    r0[3] = wbase + (((wn * 7 + (p & 1)) ^ (g & 1)) * 16) + ((p & 1) ^ 1) * 8;
    r0[4] = wbase + (((wn * 7 + 6) ^ (g & 1)) * 16) + (p & 1) * 8;
    r0[5] = 0;
    r0[6] = (ln >> 5) * 114688 + c * 16 + ((ln >> 4) & 1) * 256;
    r0[7] = (ln >> 5) * 114688 + c * 16 + ((ln >> 4) & 1) * 8;
    const auto srdH = mk_rsrc((const char*)Hb + (size_t)(tok0 / 16 + wm * 8) * 114688 + (size_t)(f0 / 8 + wn * 7) * 256, 8 * 114688);
    const u64 oddm = __builtin_amdgcn_ballot_w64((ln >> 4) & 1);
    const int kh2 = xcd & 1, d02 = ((xcd >> 2) * 4 + (idx >> 3)) * 128;
    const auto srdW2 = mk_rsrc((const char*)wd + (size_t)kh2 * 1792 * 2048 + d02 * 2, (3584 - kh2 * 1792) * 2048 - d02 * 2);
    const u32 ldsW2 = sbase + G2_WOFF + 4 * wid * 1024;
    u32x8 tv;
    {
      const int l = ln < 35 ? ln : 0;
      const char* a; u32 st;
      if (l < 24) { const int r = l / 3, k3 = l % 3; a = (const char*)(wid == 3 ? wu : wg) + (size_t)(5 * 64 + 8 * pm + r) * 7168 + f0 * 2 + (k3 == 2 ? 223 : k3 * 128); st = 64 * 7168; }
      else { const int l0 = min(bid * 224, 57344 - 242); a = (const char*)wd + (size_t)(l0 + (wid & 1) * 11 + (l - 24)) * 128; st = 22 * 128; }
      tv[0] = 0; tv[1] = st; tv[2] = (u32)(size_t)a; tv[3] = (u32)((size_t)a >> 32);
      tv[4] = tv[5] = 0;
      tv[6] = (16 * wid + (ln >> 4)) * 2048 + (((ln & 15) ^ ((ln >> 4) << 1)) * 16);
      tv[7] = (16 * wid + (ln >> 4)) * 2048 + (((ln & 15) ^ (((ln >> 4) | 4) << 1)) * 16);
    }
    asm volatile(
      "s_nop 4\n\t"
      "s_cmp_lt_u32 %[wid], 2\n\t"
      "s_cbranch_scc1 3f\n\t"
      "s_mov_b32 s40, 0\n\t"
      "s_mov_b32 s41, 229376\n\t"
      "s_mov_b32 s43, %[lds]\n\t"
      "s_add_u32 m0, s43, 0\n\t"
      "s_nop 0\n\t"
      "buffer_load_dwordx4 v232, %[srd], s40 offen offset:0 lds\n\t"
      "buffer_load_dwordx4 v233, %[srd], s40 offen offset:1024 lds\n\t"
      "buffer_load_dwordx4 v234, %[srd], s40 offen offset:2048 lds\n\t"
      "buffer_load_dwordx4 v235, %[srd], s40 offen offset:3072 lds\n\t"
      "s_add_u32 m0, s43, 4096\n\t"
      "s_nop 0\n\t"
      "buffer_load_dwordx4 v236, %[srd], s40 offen offset:0 lds\n\t"
      "buffer_load_dwordx4 v237, %[srd], s40 offen offset:1024 lds\n\t"
      "buffer_load_dwordx4 v238, %[srd], s40 offen offset:2048 lds\n\t"
      "s_add_u32 m0, s43, 7168\n\t"
      "s_nop 0\n\t"
      "buffer_load_dwordx4 v232, %[srd], s41 offen offset:0 lds\n\t"
      "buffer_load_dwordx4 v233, %[srd], s41 offen offset:1024 lds\n\t"
      "buffer_load_dwordx4 v234, %[srd], s41 offen offset:2048 lds\n\t"
      "buffer_load_dwordx4 v235, %[srd], s41 offen offset:3072 lds\n\t"
      "s_add_u32 m0, s43, 11264\n\t"
      "s_nop 0\n\t"
      "buffer_load_dwordx4 v236, %[srd], s41 offen offset:0 lds\n\t"
      "buffer_load_dwordx4 v237, %[srd], s41 offen offset:1024 lds\n\t"
      "buffer_load_dwordx4 v238, %[srd], s41 offen offset:2048 lds\n\t"
      "s_add_u32 s40, s40, 458752\n\t"
      "s_add_u32 s41, s41, 458752\n\t"
      "s_add_u32 s43, s43, 28928\n\t"
      "s_add_u32 m0, s43, 0\n\t"
      "s_nop 0\n\t"
      "buffer_load_dwordx4 v232, %[srd], s40 offen offset:0 lds\n\t"
      "buffer_load_dwordx4 v233, %[srd], s40 offen offset:1024 lds\n\t"
      "buffer_load_dwordx4 v234, %[srd], s40 offen offset:2048 lds\n\t"
      "buffer_load_dwordx4 v235, %[srd], s40 offen offset:3072 lds\n\t"
      "s_add_u32 m0, s43, 4096\n\t"
      "s_nop 0\n\t"
      "buffer_load_dwordx4 v236, %[srd], s40 offen offset:0 lds\n\t"
      "buffer_load_dwordx4 v237, %[srd], s40 offen offset:1024 lds\n\t"
      "buffer_load_dwordx4 v238, %[srd], s40 offen offset:2048 lds\n\t"
      "s_add_u32 m0, s43, 7168\n\t"
      "s_nop 0\n\t"
      "buffer_load_dwordx4 v232, %[srd], s41 offen offset:0 lds\n\t"
      "buffer_load_dwordx4 v233, %[srd], s41 offen offset:1024 lds\n\t"
      "buffer_load_dwordx4 v234, %[srd], s41 offen offset:2048 lds\n\t"
      "buffer_load_dwordx4 v235, %[srd], s41 offen offset:3072 lds\n\t"
      "s_add_u32 m0, s43, 11264\n\t"
      "s_nop 0\n\t"
      "buffer_load_dwordx4 v236, %[srd], s41 offen offset:0 lds\n\t"
      "buffer_load_dwordx4 v237, %[srd], s41 offen offset:1024 lds\n\t"
      "buffer_load_dwordx4 v238, %[srd], s41 offen offset:2048 lds\n\t"
      "s_add_u32 s40, s40, 458752\n\t"
      "s_add_u32 s41, s41, 458752\n\t"
      "s_add_u32 s43, s43, 28928\n\t"
      "s_add_u32 m0, s43, 0\n\t"
      "s_nop 0\n\t"
      "buffer_load_dwordx4 v232, %[srd], s40 offen offset:0 lds\n\t"
      "buffer_load_dwordx4 v233, %[srd], s40 offen offset:1024 lds\n\t"
      "buffer_load_dwordx4 v234, %[srd], s40 offen offset:2048 lds\n\t"
      "buffer_load_dwordx4 v235, %[srd], s40 offen offset:3072 lds\n\t"
      "s_add_u32 m0, s43, 4096\n\t"
      "s_nop 0\n\t"
      "buffer_load_dwordx4 v236, %[srd], s40 offen offset:0 lds\n\t"
      "buffer_load_dwordx4 v237, %[srd], s40 offen offset:1024 lds\n\t"
      "buffer_load_dwordx4 v238, %[srd], s40 offen offset:2048 lds\n\t"
      "s_add_u32 m0, s43, 7168\n\t"
      "s_nop 0\n\t"
      "buffer_load_dwordx4 v232, %[srd], s41 offen offset:0 lds\n\t"
      "buffer_load_dwordx4 v233, %[srd], s41 offen offset:1024 lds\n\t"
      "buffer_load_dwordx4 v234, %[srd], s41 offen offset:2048 lds\n\t"
      "buffer_load_dwordx4 v235, %[srd], s41 offen offset:3072 lds\n\t"
      "s_add_u32 m0, s43, 11264\n\t"
      "s_nop 0\n\t"
      "buffer_load_dwordx4 v236, %[srd], s41 offen offset:0 lds\n\t"
      "buffer_load_dwordx4 v237, %[srd], s41 offen offset:1024 lds\n\t"
      "buffer_load_dwordx4 v238, %[srd], s41 offen offset:2048 lds\n\t"
      "s_add_u32 s40, s40, 458752\n\t"
      "s_add_u32 s41, s41, 458752\n\t"
      "s_add_u32 s43, s43, 28928\n\t"
      "3:\n\t"
        :: "{v[232:239]}"(vd), [srd] "s"(srd), [lds] "s"(ldsd), [wid] "s"(wid) : "memory", "scc", "s40", "s41", "s43");
    if (wid == 0) poll_flags(flagB + pm * 32, 32, tag, lane);
    BARRIER();
    asm volatile(
      "s_nop 4\n\t"
      "s_cmp_lt_u32 %[wid], 2\n\t"
      "s_cbranch_scc0 2f\n\t"
      "s_mov_b32 s40, 0\n\t"
      "s_mov_b32 s41, 131072\n\t"
      "s_mov_b32 s42, %[lds]\n\t"
      "s_add_u32 m0, s42, 0\n\t"
      "s_nop 0\n\t"
      "buffer_load_dwordx4 v232, %[srd], s40 offen offset:0 sc1 lds\n\t"
      "buffer_load_dwordx4 v233, %[srd], s40 offen offset:1024 sc1 lds\n\t"
      "buffer_load_dwordx4 v234, %[srd], s40 offen offset:2048 sc1 lds\n\t"
      "buffer_load_dwordx4 v235, %[srd], s40 offen offset:3072 sc1 lds\n\t"
      "s_add_u32 m0, s42, 4096\n\t"
      "s_nop 0\n\t"
      "buffer_load_dwordx4 v236, %[srd], s40 offen offset:0 sc1 lds\n\t"
      "buffer_load_dwordx4 v237, %[srd], s40 offen offset:1024 sc1 lds\n\t"
      "buffer_load_dwordx4 v238, %[srd], s40 offen offset:2048 sc1 lds\n\t"
      "buffer_load_dwordx4 v239, %[srd], s40 offen offset:3072 sc1 lds\n\t"
      "s_add_u32 m0, s42, 8192\n\t"
      "s_nop 0\n\t"
      "buffer_load_dwordx4 v232, %[srd], s41 offen offset:0 sc1 lds\n\t"
      "buffer_load_dwordx4 v233, %[srd], s41 offen offset:1024 sc1 lds\n\t"
      "buffer_load_dwordx4 v234, %[srd], s41 offen offset:2048 sc1 lds\n\t"
      "buffer_load_dwordx4 v235, %[srd], s41 offen offset:3072 sc1 lds\n\t"
      "s_add_u32 m0, s42, 12288\n\t"
      "s_nop 0\n\t"
      "buffer_load_dwordx4 v236, %[srd], s41 offen offset:0 sc1 lds\n\t"
      "buffer_load_dwordx4 v237, %[srd], s41 offen offset:1024 sc1 lds\n\t"
      "buffer_load_dwordx4 v238, %[srd], s41 offen offset:2048 sc1 lds\n\t"
      "buffer_load_dwordx4 v239, %[srd], s41 offen offset:3072 sc1 lds\n\t"
      "s_add_u32 s40, s40, 128\n\t"
      "s_add_u32 s41, s41, 128\n\t"
      "s_add_u32 s42, s42, 32768\n\t"
      "s_add_u32 m0, s42, 0\n\t"
      "s_nop 0\n\t"
      "buffer_load_dwordx4 v232, %[srd], s40 offen offset:0 sc1 lds\n\t"
      "buffer_load_dwordx4 v233, %[srd], s40 offen offset:1024 sc1 lds\n\t"
      "buffer_load_dwordx4 v234, %[srd], s40 offen offset:2048 sc1 lds\n\t"
      "buffer_load_dwordx4 v235, %[srd], s40 offen offset:3072 sc1 lds\n\t"
      "s_add_u32 m0, s42, 4096\n\t"
      "s_nop 0\n\t"
      "buffer_load_dwordx4 v236, %[srd], s40 offen offset:0 sc1 lds\n\t"
      "buffer_load_dwordx4 v237, %[srd], s40 offen offset:1024 sc1 lds\n\t"
      "buffer_load_dwordx4 v238, %[srd], s40 offen offset:2048 sc1 lds\n\t"
      "buffer_load_dwordx4 v239, %[srd], s40 offen offset:3072 sc1 lds\n\t"
      "s_add_u32 m0, s42, 8192\n\t"
      "s_nop 0\n\t"
      "buffer_load_dwordx4 v232, %[srd], s41 offen offset:0 sc1 lds\n\t"
      "buffer_load_dwordx4 v233, %[srd], s41 offen offset:1024 sc1 lds\n\t"
      "buffer_load_dwordx4 v234, %[srd], s41 offen offset:2048 sc1 lds\n\t"
      "buffer_load_dwordx4 v235, %[srd], s41 offen offset:3072 sc1 lds\n\t"
      "s_add_u32 m0, s42, 12288\n\t"
      "s_nop 0\n\t"
      "buffer_load_dwordx4 v236, %[srd], s41 offen offset:0 sc1 lds\n\t"
      "buffer_load_dwordx4 v237, %[srd], s41 offen offset:1024 sc1 lds\n\t"
      "buffer_load_dwordx4 v238, %[srd], s41 offen offset:2048 sc1 lds\n\t"
      "buffer_load_dwordx4 v239, %[srd], s41 offen offset:3072 sc1 lds\n\t"
      "s_add_u32 s40, s40, 128\n\t"
      "s_add_u32 s41, s41, 128\n\t"
      "s_add_u32 s42, s42, 32768\n\t"
      "s_mov_b32 s42, %[lds]\n\t"
      "s_mov_b32 s44, 0\n\t"
      "s_mov_b32 s47, 32768\n\t"
      "s_mov_b32 s48, 28928\n\t"
      "s_mov_b32 s49, -57856\n\t"
      "s_mov_b32 s51, 4096\n\t"
      "s_waitcnt vmcnt(16)\n\t"
      "s_barrier\n\t"
      "ds_read_b64_tr_b16 a[64:65], v226 offset:0\n\t"
      "ds_read_b64_tr_b16 a[66:67], v226 offset:896\n\t"
      "ds_read_b64_tr_b16 a[68:69], v227 offset:0\n\t"
      "ds_read_b64_tr_b16 a[70:71], v227 offset:896\n\t"
      "ds_read_b64_tr_b16 a[72:73], v226 offset:32\n\t"
      "ds_read_b64_tr_b16 a[74:75], v226 offset:928\n\t"
      "ds_read_b64_tr_b16 a[76:77], v227 offset:32\n\t"
      "ds_read_b64_tr_b16 a[78:79], v227 offset:928\n\t"
      "ds_read_b64_tr_b16 a[80:81], v226 offset:64\n\t"
      "ds_read_b64_tr_b16 a[82:83], v226 offset:960\n\t"
      "ds_read_b64_tr_b16 a[84:85], v227 offset:64\n\t"
      "ds_read_b64_tr_b16 a[86:87], v227 offset:960\n\t"
      "ds_read_b64_tr_b16 a[88:89], v228 offset:0\n\t"
      "ds_read_b64_tr_b16 a[90:91], v228 offset:896\n\t"
      "ds_read_b128 a[0:3], v224 offset:0\n\t"
      "ds_read_b128 a[4:7], v224 offset:2048\n\t"
      "ds_read_b128 a[8:11], v224 offset:4096\n\t"
      "ds_read_b128 a[12:15], v224 offset:6144\n\t"
      "ds_read_b128 a[16:19], v224 offset:8192\n\t"
      "ds_read_b128 a[20:23], v224 offset:10240\n\t"
      "ds_read_b128 a[24:27], v224 offset:12288\n\t"
      "ds_read_b128 a[28:31], v224 offset:14336\n\t"
      "s_waitcnt lgkmcnt(0)\n\t"
      "s_mov_b32 s45, 14\n\t"
      "1:\n\t"
      "v_mfma_f32_16x16x32_bf16 v[0:3], a[64:67], a[0:3], v[0:3]\n\t"
      "ds_read_b64_tr_b16 a[92:93], v226 offset:7168\n\t"
      "v_mfma_f32_16x16x32_bf16 v[4:7], a[68:71], a[0:3], v[4:7]\n\t"
      "ds_read_b64_tr_b16 a[94:95], v226 offset:8064\n\t"
      "v_mfma_f32_16x16x32_bf16 v[8:11], a[72:75], a[0:3], v[8:11]\n\t"
      "ds_read_b64_tr_b16 a[96:97], v227 offset:7168\n\t"
      "v_mfma_f32_16x16x32_bf16 v[12:15], a[76:79], a[0:3], v[12:15]\n\t"
      "ds_read_b64_tr_b16 a[98:99], v227 offset:8064\n\t"
      "v_mfma_f32_16x16x32_bf16 v[16:19], a[80:83], a[0:3], v[16:19]\n\t"
      "ds_read_b64_tr_b16 a[100:101], v226 offset:7200\n\t"
      "v_mfma_f32_16x16x32_bf16 v[20:23], a[84:87], a[0:3], v[20:23]\n\t"
      "ds_read_b64_tr_b16 a[102:103], v226 offset:8096\n\t"
      "v_mfma_f32_16x16x32_bf16 v[24:27], a[88:91], a[0:3], v[24:27]\n\t"
      "ds_read_b64_tr_b16 a[104:105], v227 offset:7200\n\t"
      "v_mfma_f32_16x16x32_bf16 v[52:55], a[88:91], a[4:7], v[52:55]\n\t"
      "ds_read_b64_tr_b16 a[106:107], v227 offset:8096\n\t"
      "v_mfma_f32_16x16x32_bf16 v[48:51], a[84:87], a[4:7], v[48:51]\n\t"
      "ds_read_b64_tr_b16 a[108:109], v226 offset:7232\n\t"
      "v_mfma_f32_16x16x32_bf16 v[44:47], a[80:83], a[4:7], v[44:47]\n\t"
      "ds_read_b64_tr_b16 a[110:111], v226 offset:8128\n\t"
      "v_mfma_f32_16x16x32_bf16 v[40:43], a[76:79], a[4:7], v[40:43]\n\t"
      "ds_read_b64_tr_b16 a[112:113], v227 offset:7232\n\t"
      "v_mfma_f32_16x16x32_bf16 v[36:39], a[72:75], a[4:7], v[36:39]\n\t"
      "ds_read_b64_tr_b16 a[114:115], v227 offset:8128\n\t"
      "v_mfma_f32_16x16x32_bf16 v[32:35], a[68:71], a[4:7], v[32:35]\n\t"
      "ds_read_b64_tr_b16 a[116:117], v228 offset:7168\n\t"
      "v_mfma_f32_16x16x32_bf16 v[28:31], a[64:67], a[4:7], v[28:31]\n\t"
      "ds_read_b64_tr_b16 a[118:119], v228 offset:8064\n\t"
      "v_mfma_f32_16x16x32_bf16 v[56:59], a[64:67], a[8:11], v[56:59]\n\t"
      "ds_read_b128 a[32:35], v225 offset:0\n\t"
      "v_mfma_f32_16x16x32_bf16 v[60:63], a[68:71], a[8:11], v[60:63]\n\t"
      "ds_read_b128 a[36:39], v225 offset:2048\n\t"
      "v_mfma_f32_16x16x32_bf16 v[64:67], a[72:75], a[8:11], v[64:67]\n\t"
      "ds_read_b128 a[40:43], v225 offset:4096\n\t"
      "v_mfma_f32_16x16x32_bf16 v[68:71], a[76:79], a[8:11], v[68:71]\n\t"
      "ds_read_b128 a[44:47], v225 offset:6144\n\t"
      "v_mfma_f32_16x16x32_bf16 v[72:75], a[80:83], a[8:11], v[72:75]\n\t"
      "ds_read_b128 a[48:51], v225 offset:8192\n\t"
      "v_mfma_f32_16x16x32_bf16 v[76:79], a[84:87], a[8:11], v[76:79]\n\t"
      "ds_read_b128 a[52:55], v225 offset:10240\n\t"
      "v_mfma_f32_16x16x32_bf16 v[80:83], a[88:91], a[8:11], v[80:83]\n\t"
      "ds_read_b128 a[56:59], v225 offset:12288\n\t"
      "v_mfma_f32_16x16x32_bf16 v[108:111], a[88:91], a[12:15], v[108:111]\n\t"
      "ds_read_b128 a[60:63], v225 offset:14336\n\t"
      "v_mfma_f32_16x16x32_bf16 v[104:107], a[84:87], a[12:15], v[104:107]\n\t"
      "s_add_u32 s44, s44, 1\n\t"
      "v_mfma_f32_16x16x32_bf16 v[100:103], a[80:83], a[12:15], v[100:103]\n\t"
      "s_cmp_eq_u32 s44, 3\n\t"
      "s_cselect_b32 s46, s49, s48\n\t"
      "s_cselect_b32 s44, 0, s44\n\t"
      "v_mfma_f32_16x16x32_bf16 v[96:99], a[76:79], a[12:15], v[96:99]\n\t"
      "v_add_u32 v224, s47, v224\n\t"
      "v_mfma_f32_16x16x32_bf16 v[92:95], a[72:75], a[12:15], v[92:95]\n\t"
      "v_add_u32 v225, s47, v225\n\t"
      "v_mfma_f32_16x16x32_bf16 v[88:91], a[68:71], a[12:15], v[88:91]\n\t"
      "v_add_u32 v226, s46, v226\n\t"
      "v_mfma_f32_16x16x32_bf16 v[84:87], a[64:67], a[12:15], v[84:87]\n\t"
      "v_add_u32 v227, s46, v227\n\t"
      "v_mfma_f32_16x16x32_bf16 v[112:115], a[64:67], a[16:19], v[112:115]\n\t"
      "v_add_u32 v228, s46, v228\n\t"
      "v_mfma_f32_16x16x32_bf16 v[116:119], a[68:71], a[16:19], v[116:119]\n\t"
      "v_mfma_f32_16x16x32_bf16 v[120:123], a[72:75], a[16:19], v[120:123]\n\t"
      "s_waitcnt vmcnt(0) lgkmcnt(0)\n\t"
      "s_barrier\n\t"
      "v_mfma_f32_16x16x32_bf16 v[124:127], a[76:79], a[16:19], v[124:127]\n\t"
      "s_add_u32 m0, s42, 0\n\t"
      "v_mfma_f32_16x16x32_bf16 v[128:131], a[80:83], a[16:19], v[128:131]\n\t"
      "buffer_load_dwordx4 v232, %[srd], s40 offen offset:0 sc1 lds\n\t"
      "v_mfma_f32_16x16x32_bf16 v[132:135], a[84:87], a[16:19], v[132:135]\n\t"
      "v_mfma_f32_16x16x32_bf16 v[136:139], a[88:91], a[16:19], v[136:139]\n\t"
      "buffer_load_dwordx4 v233, %[srd], s40 offen offset:1024 sc1 lds\n\t"
      "v_mfma_f32_16x16x32_bf16 v[164:167], a[88:91], a[20:23], v[164:167]\n\t"
      "v_mfma_f32_16x16x32_bf16 v[160:163], a[84:87], a[20:23], v[160:163]\n\t"
      "v_mfma_f32_16x16x32_bf16 v[156:159], a[80:83], a[20:23], v[156:159]\n\t"
      "buffer_load_dwordx4 v234, %[srd], s40 offen offset:2048 sc1 lds\n\t"
      "v_mfma_f32_16x16x32_bf16 v[152:155], a[76:79], a[20:23], v[152:155]\n\t"
      "v_mfma_f32_16x16x32_bf16 v[148:151], a[72:75], a[20:23], v[148:151]\n\t"
      "v_mfma_f32_16x16x32_bf16 v[144:147], a[68:71], a[20:23], v[144:147]\n\t"
      "buffer_load_dwordx4 v235, %[srd], s40 offen offset:3072 sc1 lds\n\t"
      "v_mfma_f32_16x16x32_bf16 v[140:143], a[64:67], a[20:23], v[140:143]\n\t"
      "v_mfma_f32_16x16x32_bf16 v[168:171], a[64:67], a[24:27], v[168:171]\n\t"
      "s_add_u32 m0, s42, 4096\n\t"
      "v_mfma_f32_16x16x32_bf16 v[172:175], a[68:71], a[24:27], v[172:175]\n\t"
      "buffer_load_dwordx4 v236, %[srd], s40 offen offset:0 sc1 lds\n\t"
      "v_mfma_f32_16x16x32_bf16 v[176:179], a[72:75], a[24:27], v[176:179]\n\t"
      "v_mfma_f32_16x16x32_bf16 v[180:183], a[76:79], a[24:27], v[180:183]\n\t"
      "v_mfma_f32_16x16x32_bf16 v[184:187], a[80:83], a[24:27], v[184:187]\n\t"
      "buffer_load_dwordx4 v237, %[srd], s40 offen offset:1024 sc1 lds\n\t"
      "v_mfma_f32_16x16x32_bf16 v[188:191], a[84:87], a[24:27], v[188:191]\n\t"
      "v_mfma_f32_16x16x32_bf16 v[192:195], a[88:91], a[24:27], v[192:195]\n\t"
      "buffer_load_dwordx4 v238, %[srd], s40 offen offset:2048 sc1 lds\n\t"
      "v_mfma_f32_16x16x32_bf16 v[220:223], a[88:91], a[28:31], v[220:223]\n\t"
      "v_mfma_f32_16x16x32_bf16 v[216:219], a[84:87], a[28:31], v[216:219]\n\t"
      "v_mfma_f32_16x16x32_bf16 v[212:215], a[80:83], a[28:31], v[212:215]\n\t"
      "buffer_load_dwordx4 v239, %[srd], s40 offen offset:3072 sc1 lds\n\t"
      "v_mfma_f32_16x16x32_bf16 v[208:211], a[76:79], a[28:31], v[208:211]\n\t"
      "v_mfma_f32_16x16x32_bf16 v[204:207], a[72:75], a[28:31], v[204:207]\n\t"
      "s_add_u32 m0, s42, 8192\n\t"
      "v_mfma_f32_16x16x32_bf16 v[200:203], a[68:71], a[28:31], v[200:203]\n\t"
      "buffer_load_dwordx4 v232, %[srd], s41 offen offset:0 sc1 lds\n\t"
      "v_mfma_f32_16x16x32_bf16 v[196:199], a[64:67], a[28:31], v[196:199]\n\t"
      "v_mfma_f32_16x16x32_bf16 v[0:3], a[92:95], a[32:35], v[0:3]\n\t"
      "v_mfma_f32_16x16x32_bf16 v[4:7], a[96:99], a[32:35], v[4:7]\n\t"
      "buffer_load_dwordx4 v233, %[srd], s41 offen offset:1024 sc1 lds\n\t"
      "v_mfma_f32_16x16x32_bf16 v[8:11], a[100:103], a[32:35], v[8:11]\n\t"
      "v_mfma_f32_16x16x32_bf16 v[12:15], a[104:107], a[32:35], v[12:15]\n\t"
      "v_mfma_f32_16x16x32_bf16 v[16:19], a[108:111], a[32:35], v[16:19]\n\t"
      "buffer_load_dwordx4 v234, %[srd], s41 offen offset:2048 sc1 lds\n\t"
      "v_mfma_f32_16x16x32_bf16 v[20:23], a[112:115], a[32:35], v[20:23]\n\t"
      "v_mfma_f32_16x16x32_bf16 v[24:27], a[116:119], a[32:35], v[24:27]\n\t"
      "buffer_load_dwordx4 v235, %[srd], s41 offen offset:3072 sc1 lds\n\t"
      "v_mfma_f32_16x16x32_bf16 v[52:55], a[116:119], a[36:39], v[52:55]\n\t"
      "v_mfma_f32_16x16x32_bf16 v[48:51], a[112:115], a[36:39], v[48:51]\n\t"
      "s_add_u32 m0, s42, 12288\n\t"
      "v_mfma_f32_16x16x32_bf16 v[44:47], a[108:111], a[36:39], v[44:47]\n\t"
      "buffer_load_dwordx4 v236, %[srd], s41 offen offset:0 sc1 lds\n\t"
      "v_mfma_f32_16x16x32_bf16 v[40:43], a[104:107], a[36:39], v[40:43]\n\t"
      "v_mfma_f32_16x16x32_bf16 v[36:39], a[100:103], a[36:39], v[36:39]\n\t"
      "v_mfma_f32_16x16x32_bf16 v[32:35], a[96:99], a[36:39], v[32:35]\n\t"
      "buffer_load_dwordx4 v237, %[srd], s41 offen offset:1024 sc1 lds\n\t"
      "v_mfma_f32_16x16x32_bf16 v[28:31], a[92:95], a[36:39], v[28:31]\n\t"
      "v_mfma_f32_16x16x32_bf16 v[56:59], a[92:95], a[40:43], v[56:59]\n\t"
      "v_mfma_f32_16x16x32_bf16 v[60:63], a[96:99], a[40:43], v[60:63]\n\t"
      "buffer_load_dwordx4 v238, %[srd], s41 offen offset:2048 sc1 lds\n\t"
      "v_mfma_f32_16x16x32_bf16 v[64:67], a[100:103], a[40:43], v[64:67]\n\t"
      "v_mfma_f32_16x16x32_bf16 v[68:71], a[104:107], a[40:43], v[68:71]\n\t"
      "v_mfma_f32_16x16x32_bf16 v[72:75], a[108:111], a[40:43], v[72:75]\n\t"
      "buffer_load_dwordx4 v239, %[srd], s41 offen offset:3072 sc1 lds\n\t"
      "v_mfma_f32_16x16x32_bf16 v[76:79], a[112:115], a[40:43], v[76:79]\n\t"
      "s_add_u32 s40, s40, 128\n\t"
      "v_mfma_f32_16x16x32_bf16 v[80:83], a[116:119], a[40:43], v[80:83]\n\t"
      "ds_read_b64_tr_b16 a[64:65], v226 offset:0\n\t"
      "s_add_u32 s41, s41, 128\n\t"
      "v_mfma_f32_16x16x32_bf16 v[108:111], a[116:119], a[44:47], v[108:111]\n\t"
      "ds_read_b64_tr_b16 a[66:67], v226 offset:896\n\t"
      "s_add_u32 s42, s42, s47\n\t"
      "v_mfma_f32_16x16x32_bf16 v[104:107], a[112:115], a[44:47], v[104:107]\n\t"
      "ds_read_b64_tr_b16 a[68:69], v227 offset:0\n\t"
      "v_mfma_f32_16x16x32_bf16 v[100:103], a[108:111], a[44:47], v[100:103]\n\t"
      "ds_read_b64_tr_b16 a[70:71], v227 offset:896\n\t"
      "v_mfma_f32_16x16x32_bf16 v[96:99], a[104:107], a[44:47], v[96:99]\n\t"
      "ds_read_b64_tr_b16 a[72:73], v226 offset:32\n\t"
      "v_mfma_f32_16x16x32_bf16 v[92:95], a[100:103], a[44:47], v[92:95]\n\t"
      "ds_read_b64_tr_b16 a[74:75], v226 offset:928\n\t"
      "v_mfma_f32_16x16x32_bf16 v[88:91], a[96:99], a[44:47], v[88:91]\n\t"
      "ds_read_b64_tr_b16 a[76:77], v227 offset:32\n\t"
      "v_mfma_f32_16x16x32_bf16 v[84:87], a[92:95], a[44:47], v[84:87]\n\t"
      "ds_read_b64_tr_b16 a[78:79], v227 offset:928\n\t"
      "v_mfma_f32_16x16x32_bf16 v[112:115], a[92:95], a[48:51], v[112:115]\n\t"
      "ds_read_b64_tr_b16 a[80:81], v226 offset:64\n\t"
      "v_mfma_f32_16x16x32_bf16 v[116:119], a[96:99], a[48:51], v[116:119]\n\t"
      "ds_read_b64_tr_b16 a[82:83], v226 offset:960\n\t"
      "v_mfma_f32_16x16x32_bf16 v[120:123], a[100:103], a[48:51], v[120:123]\n\t"
      "ds_read_b64_tr_b16 a[84:85], v227 offset:64\n\t"
      "v_mfma_f32_16x16x32_bf16 v[124:127], a[104:107], a[48:51], v[124:127]\n\t"
      "ds_read_b64_tr_b16 a[86:87], v227 offset:960\n\t"
      "v_mfma_f32_16x16x32_bf16 v[128:131], a[108:111], a[48:51], v[128:131]\n\t"
      "ds_read_b64_tr_b16 a[88:89], v228 offset:0\n\t"
      "v_mfma_f32_16x16x32_bf16 v[132:135], a[112:115], a[48:51], v[132:135]\n\t"
      "ds_read_b64_tr_b16 a[90:91], v228 offset:896\n\t"
      "v_mfma_f32_16x16x32_bf16 v[136:139], a[116:119], a[48:51], v[136:139]\n\t"
      "ds_read_b128 a[0:3], v224 offset:0\n\t"
      "v_mfma_f32_16x16x32_bf16 v[164:167], a[116:119], a[52:55], v[164:167]\n\t"
      "ds_read_b128 a[4:7], v224 offset:2048\n\t"
      "v_mfma_f32_16x16x32_bf16 v[160:163], a[112:115], a[52:55], v[160:163]\n\t"
      "ds_read_b128 a[8:11], v224 offset:4096\n\t"
      "v_mfma_f32_16x16x32_bf16 v[156:159], a[108:111], a[52:55], v[156:159]\n\t"
      "ds_read_b128 a[12:15], v224 offset:6144\n\t"
      "v_mfma_f32_16x16x32_bf16 v[152:155], a[104:107], a[52:55], v[152:155]\n\t"
      "ds_read_b128 a[16:19], v224 offset:8192\n\t"
      "v_mfma_f32_16x16x32_bf16 v[148:151], a[100:103], a[52:55], v[148:151]\n\t"
      "ds_read_b128 a[20:23], v224 offset:10240\n\t"
      "v_mfma_f32_16x16x32_bf16 v[144:147], a[96:99], a[52:55], v[144:147]\n\t"
      "ds_read_b128 a[24:27], v224 offset:12288\n\t"
      "v_mfma_f32_16x16x32_bf16 v[140:143], a[92:95], a[52:55], v[140:143]\n\t"
      "ds_read_b128 a[28:31], v224 offset:14336\n\t"
      "v_mfma_f32_16x16x32_bf16 v[168:171], a[92:95], a[56:59], v[168:171]\n\t"
      "v_mfma_f32_16x16x32_bf16 v[172:175], a[96:99], a[56:59], v[172:175]\n\t"
      "v_mfma_f32_16x16x32_bf16 v[176:179], a[100:103], a[56:59], v[176:179]\n\t"
      "v_mfma_f32_16x16x32_bf16 v[180:183], a[104:107], a[56:59], v[180:183]\n\t"
      "v_mfma_f32_16x16x32_bf16 v[184:187], a[108:111], a[56:59], v[184:187]\n\t"
      "v_mfma_f32_16x16x32_bf16 v[188:191], a[112:115], a[56:59], v[188:191]\n\t"
      "v_mfma_f32_16x16x32_bf16 v[192:195], a[116:119], a[56:59], v[192:195]\n\t"
      "v_mfma_f32_16x16x32_bf16 v[220:223], a[116:119], a[60:63], v[220:223]\n\t"
      "v_mfma_f32_16x16x32_bf16 v[216:219], a[112:115], a[60:63], v[216:219]\n\t"
      "v_mfma_f32_16x16x32_bf16 v[212:215], a[108:111], a[60:63], v[212:215]\n\t"
      "v_mfma_f32_16x16x32_bf16 v[208:211], a[104:107], a[60:63], v[208:211]\n\t"
      "s_sub_u32 s47, 0, s47\n\t"
      "v_mfma_f32_16x16x32_bf16 v[204:207], a[100:103], a[60:63], v[204:207]\n\t"
      "v_mfma_f32_16x16x32_bf16 v[200:203], a[96:99], a[60:63], v[200:203]\n\t"
      "s_waitcnt lgkmcnt(0)\n\t"
      "v_mfma_f32_16x16x32_bf16 v[196:199], a[92:95], a[60:63], v[196:199]\n\t"
      "s_sub_u32 s45, s45, 1\n\t"
      "s_cmp_lg_u32 s45, 0\n\t"
      "s_cbranch_scc1 1b\n\t"
      "v_mfma_f32_16x16x32_bf16 v[0:3], a[64:67], a[0:3], v[0:3]\n\t"
      "ds_read_b64_tr_b16 a[92:93], v226 offset:7168\n\t"
      "v_mfma_f32_16x16x32_bf16 v[4:7], a[68:71], a[0:3], v[4:7]\n\t"
      "ds_read_b64_tr_b16 a[94:95], v226 offset:8064\n\t"
      "v_mfma_f32_16x16x32_bf16 v[8:11], a[72:75], a[0:3], v[8:11]\n\t"
      "ds_read_b64_tr_b16 a[96:97], v227 offset:7168\n\t"
      "v_mfma_f32_16x16x32_bf16 v[12:15], a[76:79], a[0:3], v[12:15]\n\t"
      "ds_read_b64_tr_b16 a[98:99], v227 offset:8064\n\t"
      "v_mfma_f32_16x16x32_bf16 v[16:19], a[80:83], a[0:3], v[16:19]\n\t"
      "ds_read_b64_tr_b16 a[100:101], v226 offset:7200\n\t"
      "v_mfma_f32_16x16x32_bf16 v[20:23], a[84:87], a[0:3], v[20:23]\n\t"
      "ds_read_b64_tr_b16 a[102:103], v226 offset:8096\n\t"
      "v_mfma_f32_16x16x32_bf16 v[24:27], a[88:91], a[0:3], v[24:27]\n\t"
      "ds_read_b64_tr_b16 a[104:105], v227 offset:7200\n\t"
      "v_mfma_f32_16x16x32_bf16 v[52:55], a[88:91], a[4:7], v[52:55]\n\t"
      "ds_read_b64_tr_b16 a[106:107], v227 offset:8096\n\t"
      "v_mfma_f32_16x16x32_bf16 v[48:51], a[84:87], a[4:7], v[48:51]\n\t"
      "ds_read_b64_tr_b16 a[108:109], v226 offset:7232\n\t"
      "v_mfma_f32_16x16x32_bf16 v[44:47], a[80:83], a[4:7], v[44:47]\n\t"
      "ds_read_b64_tr_b16 a[110:111], v226 offset:8128\n\t"
      "v_mfma_f32_16x16x32_bf16 v[40:43], a[76:79], a[4:7], v[40:43]\n\t"
      "ds_read_b64_tr_b16 a[112:113], v227 offset:7232\n\t"
      "v_mfma_f32_16x16x32_bf16 v[36:39], a[72:75], a[4:7], v[36:39]\n\t"
      "ds_read_b64_tr_b16 a[114:115], v227 offset:8128\n\t"
      "v_mfma_f32_16x16x32_bf16 v[32:35], a[68:71], a[4:7], v[32:35]\n\t"
      "ds_read_b64_tr_b16 a[116:117], v228 offset:7168\n\t"
      "v_mfma_f32_16x16x32_bf16 v[28:31], a[64:67], a[4:7], v[28:31]\n\t"
      "ds_read_b64_tr_b16 a[118:119], v228 offset:8064\n\t"
      "v_mfma_f32_16x16x32_bf16 v[56:59], a[64:67], a[8:11], v[56:59]\n\t"
      "ds_read_b128 a[32:35], v225 offset:0\n\t"
      "v_mfma_f32_16x16x32_bf16 v[60:63], a[68:71], a[8:11], v[60:63]\n\t"
      "ds_read_b128 a[36:39], v225 offset:2048\n\t"
      "v_mfma_f32_16x16x32_bf16 v[64:67], a[72:75], a[8:11], v[64:67]\n\t"
      "ds_read_b128 a[40:43], v225 offset:4096\n\t"
      "v_mfma_f32_16x16x32_bf16 v[68:71], a[76:79], a[8:11], v[68:71]\n\t"
      "ds_read_b128 a[44:47], v225 offset:6144\n\t"
      "v_mfma_f32_16x16x32_bf16 v[72:75], a[80:83], a[8:11], v[72:75]\n\t"
      "ds_read_b128 a[48:51], v225 offset:8192\n\t"
      "v_mfma_f32_16x16x32_bf16 v[76:79], a[84:87], a[8:11], v[76:79]\n\t"
      "ds_read_b128 a[52:55], v225 offset:10240\n\t"
      "v_mfma_f32_16x16x32_bf16 v[80:83], a[88:91], a[8:11], v[80:83]\n\t"
      "ds_read_b128 a[56:59], v225 offset:12288\n\t"
      "v_mfma_f32_16x16x32_bf16 v[108:111], a[88:91], a[12:15], v[108:111]\n\t"
      "ds_read_b128 a[60:63], v225 offset:14336\n\t"
      "v_mfma_f32_16x16x32_bf16 v[104:107], a[84:87], a[12:15], v[104:107]\n\t"
      "s_add_u32 s44, s44, 1\n\t"
      "v_mfma_f32_16x16x32_bf16 v[100:103], a[80:83], a[12:15], v[100:103]\n\t"
      "s_cmp_eq_u32 s44, 3\n\t"
      "s_cselect_b32 s46, s49, s48\n\t"
      "s_cselect_b32 s44, 0, s44\n\t"
      "v_mfma_f32_16x16x32_bf16 v[96:99], a[76:79], a[12:15], v[96:99]\n\t"
      "v_add_u32 v224, s47, v224\n\t"
      "v_mfma_f32_16x16x32_bf16 v[92:95], a[72:75], a[12:15], v[92:95]\n\t"
      "v_add_u32 v225, s47, v225\n\t"
      "v_mfma_f32_16x16x32_bf16 v[88:91], a[68:71], a[12:15], v[88:91]\n\t"
      "v_add_u32 v226, s46, v226\n\t"
      "v_mfma_f32_16x16x32_bf16 v[84:87], a[64:67], a[12:15], v[84:87]\n\t"
      "v_add_u32 v227, s46, v227\n\t"
      "v_mfma_f32_16x16x32_bf16 v[112:115], a[64:67], a[16:19], v[112:115]\n\t"
      "v_add_u32 v228, s46, v228\n\t"
      "v_mfma_f32_16x16x32_bf16 v[116:119], a[68:71], a[16:19], v[116:119]\n\t"
      "v_mfma_f32_16x16x32_bf16 v[120:123], a[72:75], a[16:19], v[120:123]\n\t"
      "s_waitcnt vmcnt(0) lgkmcnt(0)\n\t"
      "s_barrier\n\t"
      "v_mfma_f32_16x16x32_bf16 v[124:127], a[76:79], a[16:19], v[124:127]\n\t"
      "v_mfma_f32_16x16x32_bf16 v[128:131], a[80:83], a[16:19], v[128:131]\n\t"
      "v_mfma_f32_16x16x32_bf16 v[132:135], a[84:87], a[16:19], v[132:135]\n\t"
      "v_mfma_f32_16x16x32_bf16 v[136:139], a[88:91], a[16:19], v[136:139]\n\t"
      "v_mfma_f32_16x16x32_bf16 v[164:167], a[88:91], a[20:23], v[164:167]\n\t"
      "v_mfma_f32_16x16x32_bf16 v[160:163], a[84:87], a[20:23], v[160:163]\n\t"
      "v_mfma_f32_16x16x32_bf16 v[156:159], a[80:83], a[20:23], v[156:159]\n\t"
      "v_mfma_f32_16x16x32_bf16 v[152:155], a[76:79], a[20:23], v[152:155]\n\t"
      "v_mfma_f32_16x16x32_bf16 v[148:151], a[72:75], a[20:23], v[148:151]\n\t"
      "v_mfma_f32_16x16x32_bf16 v[144:147], a[68:71], a[20:23], v[144:147]\n\t"
      "v_mfma_f32_16x16x32_bf16 v[140:143], a[64:67], a[20:23], v[140:143]\n\t"
      "v_mfma_f32_16x16x32_bf16 v[168:171], a[64:67], a[24:27], v[168:171]\n\t"
      "v_mfma_f32_16x16x32_bf16 v[172:175], a[68:71], a[24:27], v[172:175]\n\t"
      "v_mfma_f32_16x16x32_bf16 v[176:179], a[72:75], a[24:27], v[176:179]\n\t"
      "v_mfma_f32_16x16x32_bf16 v[180:183], a[76:79], a[24:27], v[180:183]\n\t"
      "v_mfma_f32_16x16x32_bf16 v[184:187], a[80:83], a[24:27], v[184:187]\n\t"
      "v_mfma_f32_16x16x32_bf16 v[188:191], a[84:87], a[24:27], v[188:191]\n\t"
      "v_mfma_f32_16x16x32_bf16 v[192:195], a[88:91], a[24:27], v[192:195]\n\t"
      "v_mfma_f32_16x16x32_bf16 v[220:223], a[88:91], a[28:31], v[220:223]\n\t"
      "v_mfma_f32_16x16x32_bf16 v[216:219], a[84:87], a[28:31], v[216:219]\n\t"
      "v_mfma_f32_16x16x32_bf16 v[212:215], a[80:83], a[28:31], v[212:215]\n\t"
      "v_mfma_f32_16x16x32_bf16 v[208:211], a[76:79], a[28:31], v[208:211]\n\t"
      "v_mfma_f32_16x16x32_bf16 v[204:207], a[72:75], a[28:31], v[204:207]\n\t"
      "v_mfma_f32_16x16x32_bf16 v[200:203], a[68:71], a[28:31], v[200:203]\n\t"
      "v_mfma_f32_16x16x32_bf16 v[196:199], a[64:67], a[28:31], v[196:199]\n\t"
      "v_mfma_f32_16x16x32_bf16 v[0:3], a[92:95], a[32:35], v[0:3]\n\t"
      "v_mfma_f32_16x16x32_bf16 v[4:7], a[96:99], a[32:35], v[4:7]\n\t"
      "v_mfma_f32_16x16x32_bf16 v[8:11], a[100:103], a[32:35], v[8:11]\n\t"
      "v_mfma_f32_16x16x32_bf16 v[12:15], a[104:107], a[32:35], v[12:15]\n\t"
      "v_mfma_f32_16x16x32_bf16 v[16:19], a[108:111], a[32:35], v[16:19]\n\t"
      "v_mfma_f32_16x16x32_bf16 v[20:23], a[112:115], a[32:35], v[20:23]\n\t"
      "v_mfma_f32_16x16x32_bf16 v[24:27], a[116:119], a[32:35], v[24:27]\n\t"
      "v_mfma_f32_16x16x32_bf16 v[52:55], a[116:119], a[36:39], v[52:55]\n\t"
      "v_mfma_f32_16x16x32_bf16 v[48:51], a[112:115], a[36:39], v[48:51]\n\t"
      "v_mfma_f32_16x16x32_bf16 v[44:47], a[108:111], a[36:39], v[44:47]\n\t"
      "v_mfma_f32_16x16x32_bf16 v[40:43], a[104:107], a[36:39], v[40:43]\n\t"
      "v_mfma_f32_16x16x32_bf16 v[36:39], a[100:103], a[36:39], v[36:39]\n\t"
      "v_mfma_f32_16x16x32_bf16 v[32:35], a[96:99], a[36:39], v[32:35]\n\t"
      "v_mfma_f32_16x16x32_bf16 v[28:31], a[92:95], a[36:39], v[28:31]\n\t"
      "v_mfma_f32_16x16x32_bf16 v[56:59], a[92:95], a[40:43], v[56:59]\n\t"
      "v_mfma_f32_16x16x32_bf16 v[60:63], a[96:99], a[40:43], v[60:63]\n\t"
      "v_mfma_f32_16x16x32_bf16 v[64:67], a[100:103], a[40:43], v[64:67]\n\t"
      "v_mfma_f32_16x16x32_bf16 v[68:71], a[104:107], a[40:43], v[68:71]\n\t"
      "v_mfma_f32_16x16x32_bf16 v[72:75], a[108:111], a[40:43], v[72:75]\n\t"
      "v_mfma_f32_16x16x32_bf16 v[76:79], a[112:115], a[40:43], v[76:79]\n\t"
      "v_mfma_f32_16x16x32_bf16 v[80:83], a[116:119], a[40:43], v[80:83]\n\t"
      "ds_read_b64_tr_b16 a[64:65], v226 offset:0\n\t"
      "v_mfma_f32_16x16x32_bf16 v[108:111], a[116:119], a[44:47], v[108:111]\n\t"
      "ds_read_b64_tr_b16 a[66:67], v226 offset:896\n\t"
      "v_mfma_f32_16x16x32_bf16 v[104:107], a[112:115], a[44:47], v[104:107]\n\t"
      "ds_read_b64_tr_b16 a[68:69], v227 offset:0\n\t"
      "v_mfma_f32_16x16x32_bf16 v[100:103], a[108:111], a[44:47], v[100:103]\n\t"
      "ds_read_b64_tr_b16 a[70:71], v227 offset:896\n\t"
      "v_mfma_f32_16x16x32_bf16 v[96:99], a[104:107], a[44:47], v[96:99]\n\t"
      "ds_read_b64_tr_b16 a[72:73], v226 offset:32\n\t"
      "v_mfma_f32_16x16x32_bf16 v[92:95], a[100:103], a[44:47], v[92:95]\n\t"
      "ds_read_b64_tr_b16 a[74:75], v226 offset:928\n\t"
      "v_mfma_f32_16x16x32_bf16 v[88:91], a[96:99], a[44:47], v[88:91]\n\t"
      "ds_read_b64_tr_b16 a[76:77], v227 offset:32\n\t"
      "v_mfma_f32_16x16x32_bf16 v[84:87], a[92:95], a[44:47], v[84:87]\n\t"
      "ds_read_b64_tr_b16 a[78:79], v227 offset:928\n\t"
      "v_mfma_f32_16x16x32_bf16 v[112:115], a[92:95], a[48:51], v[112:115]\n\t"
      "ds_read_b64_tr_b16 a[80:81], v226 offset:64\n\t"
      "v_mfma_f32_16x16x32_bf16 v[116:119], a[96:99], a[48:51], v[116:119]\n\t"
      "ds_read_b64_tr_b16 a[82:83], v226 offset:960\n\t"
      "v_mfma_f32_16x16x32_bf16 v[120:123], a[100:103], a[48:51], v[120:123]\n\t"
      "ds_read_b64_tr_b16 a[84:85], v227 offset:64\n\t"
      "v_mfma_f32_16x16x32_bf16 v[124:127], a[104:107], a[48:51], v[124:127]\n\t"
      "ds_read_b64_tr_b16 a[86:87], v227 offset:960\n\t"
      "v_mfma_f32_16x16x32_bf16 v[128:131], a[108:111], a[48:51], v[128:131]\n\t"
      "ds_read_b64_tr_b16 a[88:89], v228 offset:0\n\t"
      "v_mfma_f32_16x16x32_bf16 v[132:135], a[112:115], a[48:51], v[132:135]\n\t"
      "ds_read_b64_tr_b16 a[90:91], v228 offset:896\n\t"
      "v_mfma_f32_16x16x32_bf16 v[136:139], a[116:119], a[48:51], v[136:139]\n\t"
      "ds_read_b128 a[0:3], v224 offset:0\n\t"
      "v_mfma_f32_16x16x32_bf16 v[164:167], a[116:119], a[52:55], v[164:167]\n\t"
      "ds_read_b128 a[4:7], v224 offset:2048\n\t"
      "v_mfma_f32_16x16x32_bf16 v[160:163], a[112:115], a[52:55], v[160:163]\n\t"
      "ds_read_b128 a[8:11], v224 offset:4096\n\t"
      "v_mfma_f32_16x16x32_bf16 v[156:159], a[108:111], a[52:55], v[156:159]\n\t"
      "ds_read_b128 a[12:15], v224 offset:6144\n\t"
      "v_mfma_f32_16x16x32_bf16 v[152:155], a[104:107], a[52:55], v[152:155]\n\t"
      "ds_read_b128 a[16:19], v224 offset:8192\n\t"
      "v_mfma_f32_16x16x32_bf16 v[148:151], a[100:103], a[52:55], v[148:151]\n\t"
      "ds_read_b128 a[20:23], v224 offset:10240\n\t"
      "v_mfma_f32_16x16x32_bf16 v[144:147], a[96:99], a[52:55], v[144:147]\n\t"
      "ds_read_b128 a[24:27], v224 offset:12288\n\t"
      "v_mfma_f32_16x16x32_bf16 v[140:143], a[92:95], a[52:55], v[140:143]\n\t"
      "ds_read_b128 a[28:31], v224 offset:14336\n\t"
      "v_mfma_f32_16x16x32_bf16 v[168:171], a[92:95], a[56:59], v[168:171]\n\t"
      "v_mfma_f32_16x16x32_bf16 v[172:175], a[96:99], a[56:59], v[172:175]\n\t"
      "v_mfma_f32_16x16x32_bf16 v[176:179], a[100:103], a[56:59], v[176:179]\n\t"
      "v_mfma_f32_16x16x32_bf16 v[180:183], a[104:107], a[56:59], v[180:183]\n\t"
      "v_mfma_f32_16x16x32_bf16 v[184:187], a[108:111], a[56:59], v[184:187]\n\t"
      "v_mfma_f32_16x16x32_bf16 v[188:191], a[112:115], a[56:59], v[188:191]\n\t"
      "v_mfma_f32_16x16x32_bf16 v[192:195], a[116:119], a[56:59], v[192:195]\n\t"
      "v_mfma_f32_16x16x32_bf16 v[220:223], a[116:119], a[60:63], v[220:223]\n\t"
      "v_mfma_f32_16x16x32_bf16 v[216:219], a[112:115], a[60:63], v[216:219]\n\t"
      "v_mfma_f32_16x16x32_bf16 v[212:215], a[108:111], a[60:63], v[212:215]\n\t"
      "v_mfma_f32_16x16x32_bf16 v[208:211], a[104:107], a[60:63], v[208:211]\n\t"
      "s_sub_u32 s47, 0, s47\n\t"
      "v_mfma_f32_16x16x32_bf16 v[204:207], a[100:103], a[60:63], v[204:207]\n\t"
      "v_mfma_f32_16x16x32_bf16 v[200:203], a[96:99], a[60:63], v[200:203]\n\t"
      "s_waitcnt lgkmcnt(0)\n\t"
      "v_mfma_f32_16x16x32_bf16 v[196:199], a[92:95], a[60:63], v[196:199]\n\t"
      "s_branch 9f\n\t"
      "2:\n\t"
      "s_mov_b32 s40, 1376256\n\t"
      "s_mov_b32 s41, 1605632\n\t"
      "s_mov_b32 s43, %[lds]\n\t"
      "s_mov_b32 s44, 0\n\t"
      "s_mov_b32 s47, 32768\n\t"
      "s_mov_b32 s48, 28928\n\t"
      "s_mov_b32 s49, -57856\n\t"
      "s_mov_b32 s51, 4096\n\t"
      "s_waitcnt vmcnt(28)\n\t"
      "s_barrier\n\t"
      "ds_read_b64_tr_b16 a[64:65], v226 offset:0\n\t"
      "ds_read_b64_tr_b16 a[66:67], v226 offset:896\n\t"
      "ds_read_b64_tr_b16 a[68:69], v227 offset:0\n\t"
      "ds_read_b64_tr_b16 a[70:71], v227 offset:896\n\t"
      "ds_read_b64_tr_b16 a[72:73], v226 offset:32\n\t"
      "ds_read_b64_tr_b16 a[74:75], v226 offset:928\n\t"
      "ds_read_b64_tr_b16 a[76:77], v227 offset:32\n\t"
      "ds_read_b64_tr_b16 a[78:79], v227 offset:928\n\t"
      "ds_read_b64_tr_b16 a[80:81], v226 offset:64\n\t"
      "ds_read_b64_tr_b16 a[82:83], v226 offset:960\n\t"
      "ds_read_b64_tr_b16 a[84:85], v227 offset:64\n\t"
      "ds_read_b64_tr_b16 a[86:87], v227 offset:960\n\t"
      "ds_read_b64_tr_b16 a[88:89], v228 offset:0\n\t"
      "ds_read_b64_tr_b16 a[90:91], v228 offset:896\n\t"
      "ds_read_b128 a[0:3], v224 offset:0\n\t"
      "ds_read_b128 a[4:7], v224 offset:2048\n\t"
      "ds_read_b128 a[8:11], v224 offset:4096\n\t"
      "ds_read_b128 a[12:15], v224 offset:6144\n\t"
      "ds_read_b128 a[16:19], v224 offset:8192\n\t"
      "ds_read_b128 a[20:23], v224 offset:10240\n\t"
      "ds_read_b128 a[24:27], v224 offset:12288\n\t"
      "ds_read_b128 a[28:31], v224 offset:14336\n\t"
      "s_waitcnt lgkmcnt(0)\n\t"
      "v_mfma_f32_16x16x32_bf16 v[0:3], a[64:67], a[0:3], v[0:3]\n\t"
      "ds_read_b64_tr_b16 a[92:93], v226 offset:7168\n\t"
      "v_mfma_f32_16x16x32_bf16 v[4:7], a[68:71], a[0:3], v[4:7]\n\t"
      "ds_read_b64_tr_b16 a[94:95], v226 offset:8064\n\t"
      "v_mfma_f32_16x16x32_bf16 v[8:11], a[72:75], a[0:3], v[8:11]\n\t"
      "ds_read_b64_tr_b16 a[96:97], v227 offset:7168\n\t"
      "v_mfma_f32_16x16x32_bf16 v[12:15], a[76:79], a[0:3], v[12:15]\n\t"
      "ds_read_b64_tr_b16 a[98:99], v227 offset:8064\n\t"
      "v_mfma_f32_16x16x32_bf16 v[16:19], a[80:83], a[0:3], v[16:19]\n\t"
      "ds_read_b64_tr_b16 a[100:101], v226 offset:7200\n\t"
      "v_mfma_f32_16x16x32_bf16 v[20:23], a[84:87], a[0:3], v[20:23]\n\t"
      "ds_read_b64_tr_b16 a[102:103], v226 offset:8096\n\t"
      "v_mfma_f32_16x16x32_bf16 v[24:27], a[88:91], a[0:3], v[24:27]\n\t"
      "ds_read_b64_tr_b16 a[104:105], v227 offset:7200\n\t"
      "v_mfma_f32_16x16x32_bf16 v[52:55], a[88:91], a[4:7], v[52:55]\n\t"
      "ds_read_b64_tr_b16 a[106:107], v227 offset:8096\n\t"
      "v_mfma_f32_16x16x32_bf16 v[48:51], a[84:87], a[4:7], v[48:51]\n\t"
      "ds_read_b64_tr_b16 a[108:109], v226 offset:7232\n\t"
      "v_mfma_f32_16x16x32_bf16 v[44:47], a[80:83], a[4:7], v[44:47]\n\t"
      "ds_read_b64_tr_b16 a[110:111], v226 offset:8128\n\t"
      "v_mfma_f32_16x16x32_bf16 v[40:43], a[76:79], a[4:7], v[40:43]\n\t"
      "ds_read_b64_tr_b16 a[112:113], v227 offset:7232\n\t"
      "v_mfma_f32_16x16x32_bf16 v[36:39], a[72:75], a[4:7], v[36:39]\n\t"
      "ds_read_b64_tr_b16 a[114:115], v227 offset:8128\n\t"
      "v_mfma_f32_16x16x32_bf16 v[32:35], a[68:71], a[4:7], v[32:35]\n\t"
      "ds_read_b64_tr_b16 a[116:117], v228 offset:7168\n\t"
      "v_mfma_f32_16x16x32_bf16 v[28:31], a[64:67], a[4:7], v[28:31]\n\t"
      "ds_read_b64_tr_b16 a[118:119], v228 offset:8064\n\t"
      "v_mfma_f32_16x16x32_bf16 v[56:59], a[64:67], a[8:11], v[56:59]\n\t"
      "ds_read_b128 a[32:35], v225 offset:0\n\t"
      "v_mfma_f32_16x16x32_bf16 v[60:63], a[68:71], a[8:11], v[60:63]\n\t"
      "ds_read_b128 a[36:39], v225 offset:2048\n\t"
      "v_mfma_f32_16x16x32_bf16 v[64:67], a[72:75], a[8:11], v[64:67]\n\t"
      "ds_read_b128 a[40:43], v225 offset:4096\n\t"
      "v_mfma_f32_16x16x32_bf16 v[68:71], a[76:79], a[8:11], v[68:71]\n\t"
      "ds_read_b128 a[44:47], v225 offset:6144\n\t"
      "v_mfma_f32_16x16x32_bf16 v[72:75], a[80:83], a[8:11], v[72:75]\n\t"
      "ds_read_b128 a[48:51], v225 offset:8192\n\t"
      "v_mfma_f32_16x16x32_bf16 v[76:79], a[84:87], a[8:11], v[76:79]\n\t"
      "ds_read_b128 a[52:55], v225 offset:10240\n\t"
      "v_mfma_f32_16x16x32_bf16 v[80:83], a[88:91], a[8:11], v[80:83]\n\t"
      "ds_read_b128 a[56:59], v225 offset:12288\n\t"
      "v_mfma_f32_16x16x32_bf16 v[108:111], a[88:91], a[12:15], v[108:111]\n\t"
      "ds_read_b128 a[60:63], v225 offset:14336\n\t"
      "v_mfma_f32_16x16x32_bf16 v[104:107], a[84:87], a[12:15], v[104:107]\n\t"
      "s_add_u32 s44, s44, 1\n\t"
      "v_mfma_f32_16x16x32_bf16 v[100:103], a[80:83], a[12:15], v[100:103]\n\t"
      "s_cmp_eq_u32 s44, 3\n\t"
      "s_cselect_b32 s46, s49, s48\n\t"
      "s_cselect_b32 s44, 0, s44\n\t"
      "v_mfma_f32_16x16x32_bf16 v[96:99], a[76:79], a[12:15], v[96:99]\n\t"
      "v_add_u32 v224, s47, v224\n\t"
      "v_mfma_f32_16x16x32_bf16 v[92:95], a[72:75], a[12:15], v[92:95]\n\t"
      "v_add_u32 v225, s47, v225\n\t"
      "v_mfma_f32_16x16x32_bf16 v[88:91], a[68:71], a[12:15], v[88:91]\n\t"
      "v_add_u32 v226, s46, v226\n\t"
      "v_mfma_f32_16x16x32_bf16 v[84:87], a[64:67], a[12:15], v[84:87]\n\t"
      "v_add_u32 v227, s46, v227\n\t"
      "v_mfma_f32_16x16x32_bf16 v[112:115], a[64:67], a[16:19], v[112:115]\n\t"
      "v_add_u32 v228, s46, v228\n\t"
      "v_mfma_f32_16x16x32_bf16 v[116:119], a[68:71], a[16:19], v[116:119]\n\t"
      "v_mfma_f32_16x16x32_bf16 v[120:123], a[72:75], a[16:19], v[120:123]\n\t"
      "s_waitcnt vmcnt(14) lgkmcnt(0)\n\t"
      "s_barrier\n\t"
      "v_mfma_f32_16x16x32_bf16 v[124:127], a[76:79], a[16:19], v[124:127]\n\t"
      "v_mfma_f32_16x16x32_bf16 v[128:131], a[80:83], a[16:19], v[128:131]\n\t"
      "s_add_u32 m0, s43, 0\n\t"
      "v_mfma_f32_16x16x32_bf16 v[132:135], a[84:87], a[16:19], v[132:135]\n\t"
      "buffer_load_dwordx4 v232, %[srd], s40 offen offset:0 lds\n\t"
      "v_mfma_f32_16x16x32_bf16 v[136:139], a[88:91], a[16:19], v[136:139]\n\t"
      "v_mfma_f32_16x16x32_bf16 v[164:167], a[88:91], a[20:23], v[164:167]\n\t"
      "v_mfma_f32_16x16x32_bf16 v[160:163], a[84:87], a[20:23], v[160:163]\n\t"
      "v_mfma_f32_16x16x32_bf16 v[156:159], a[80:83], a[20:23], v[156:159]\n\t"
      "v_mfma_f32_16x16x32_bf16 v[152:155], a[76:79], a[20:23], v[152:155]\n\t"
      "buffer_load_dwordx4 v233, %[srd], s40 offen offset:1024 lds\n\t"
      "v_mfma_f32_16x16x32_bf16 v[148:151], a[72:75], a[20:23], v[148:151]\n\t"
      "v_mfma_f32_16x16x32_bf16 v[144:147], a[68:71], a[20:23], v[144:147]\n\t"
      "v_mfma_f32_16x16x32_bf16 v[140:143], a[64:67], a[20:23], v[140:143]\n\t"
      "v_mfma_f32_16x16x32_bf16 v[168:171], a[64:67], a[24:27], v[168:171]\n\t"
      "v_mfma_f32_16x16x32_bf16 v[172:175], a[68:71], a[24:27], v[172:175]\n\t"
      "buffer_load_dwordx4 v234, %[srd], s40 offen offset:2048 lds\n\t"
      "v_mfma_f32_16x16x32_bf16 v[176:179], a[72:75], a[24:27], v[176:179]\n\t"
      "v_mfma_f32_16x16x32_bf16 v[180:183], a[76:79], a[24:27], v[180:183]\n\t"
      "v_mfma_f32_16x16x32_bf16 v[184:187], a[80:83], a[24:27], v[184:187]\n\t"
      "v_mfma_f32_16x16x32_bf16 v[188:191], a[84:87], a[24:27], v[188:191]\n\t"
      "v_mfma_f32_16x16x32_bf16 v[192:195], a[88:91], a[24:27], v[192:195]\n\t"
      "buffer_load_dwordx4 v235, %[srd], s40 offen offset:3072 lds\n\t"
      "v_mfma_f32_16x16x32_bf16 v[220:223], a[88:91], a[28:31], v[220:223]\n\t"
      "v_mfma_f32_16x16x32_bf16 v[216:219], a[84:87], a[28:31], v[216:219]\n\t"
      "v_mfma_f32_16x16x32_bf16 v[212:215], a[80:83], a[28:31], v[212:215]\n\t"
      "v_mfma_f32_16x16x32_bf16 v[208:211], a[76:79], a[28:31], v[208:211]\n\t"
      "s_add_u32 m0, s43, 4096\n\t"
      "v_mfma_f32_16x16x32_bf16 v[204:207], a[72:75], a[28:31], v[204:207]\n\t"
      "buffer_load_dwordx4 v236, %[srd], s40 offen offset:0 lds\n\t"
      "v_mfma_f32_16x16x32_bf16 v[200:203], a[68:71], a[28:31], v[200:203]\n\t"
      "v_mfma_f32_16x16x32_bf16 v[196:199], a[64:67], a[28:31], v[196:199]\n\t"
      "v_mfma_f32_16x16x32_bf16 v[0:3], a[92:95], a[32:35], v[0:3]\n\t"
      "v_mfma_f32_16x16x32_bf16 v[4:7], a[96:99], a[32:35], v[4:7]\n\t"
      "v_mfma_f32_16x16x32_bf16 v[8:11], a[100:103], a[32:35], v[8:11]\n\t"
      "buffer_load_dwordx4 v237, %[srd], s40 offen offset:1024 lds\n\t"
      "v_mfma_f32_16x16x32_bf16 v[12:15], a[104:107], a[32:35], v[12:15]\n\t"
      "v_mfma_f32_16x16x32_bf16 v[16:19], a[108:111], a[32:35], v[16:19]\n\t"
      "v_mfma_f32_16x16x32_bf16 v[20:23], a[112:115], a[32:35], v[20:23]\n\t"
      "v_mfma_f32_16x16x32_bf16 v[24:27], a[116:119], a[32:35], v[24:27]\n\t"
      "v_mfma_f32_16x16x32_bf16 v[52:55], a[116:119], a[36:39], v[52:55]\n\t"
      "buffer_load_dwordx4 v238, %[srd], s40 offen offset:2048 lds\n\t"
      "v_mfma_f32_16x16x32_bf16 v[48:51], a[112:115], a[36:39], v[48:51]\n\t"
      "v_mfma_f32_16x16x32_bf16 v[44:47], a[108:111], a[36:39], v[44:47]\n\t"
      "v_mfma_f32_16x16x32_bf16 v[40:43], a[104:107], a[36:39], v[40:43]\n\t"
      "v_mfma_f32_16x16x32_bf16 v[36:39], a[100:103], a[36:39], v[36:39]\n\t"
      "s_add_u32 m0, s43, 7168\n\t"
      "v_mfma_f32_16x16x32_bf16 v[32:35], a[96:99], a[36:39], v[32:35]\n\t"
      "buffer_load_dwordx4 v232, %[srd], s41 offen offset:0 lds\n\t"
      "v_mfma_f32_16x16x32_bf16 v[28:31], a[92:95], a[36:39], v[28:31]\n\t"
      "v_mfma_f32_16x16x32_bf16 v[56:59], a[92:95], a[40:43], v[56:59]\n\t"
      "v_mfma_f32_16x16x32_bf16 v[60:63], a[96:99], a[40:43], v[60:63]\n\t"
      "v_mfma_f32_16x16x32_bf16 v[64:67], a[100:103], a[40:43], v[64:67]\n\t"
      "v_mfma_f32_16x16x32_bf16 v[68:71], a[104:107], a[40:43], v[68:71]\n\t"
      "buffer_load_dwordx4 v233, %[srd], s41 offen offset:1024 lds\n\t"
      "v_mfma_f32_16x16x32_bf16 v[72:75], a[108:111], a[40:43], v[72:75]\n\t"
      "v_mfma_f32_16x16x32_bf16 v[76:79], a[112:115], a[40:43], v[76:79]\n\t"
      "v_mfma_f32_16x16x32_bf16 v[80:83], a[116:119], a[40:43], v[80:83]\n\t"
      "ds_read_b64_tr_b16 a[64:65], v226 offset:0\n\t"
      "v_mfma_f32_16x16x32_bf16 v[108:111], a[116:119], a[44:47], v[108:111]\n\t"
      "ds_read_b64_tr_b16 a[66:67], v226 offset:896\n\t"
      "v_mfma_f32_16x16x32_bf16 v[104:107], a[112:115], a[44:47], v[104:107]\n\t"
      "ds_read_b64_tr_b16 a[68:69], v227 offset:0\n\t"
      "buffer_load_dwordx4 v234, %[srd], s41 offen offset:2048 lds\n\t"
      "v_mfma_f32_16x16x32_bf16 v[100:103], a[108:111], a[44:47], v[100:103]\n\t"
      "ds_read_b64_tr_b16 a[70:71], v227 offset:896\n\t"
      "v_mfma_f32_16x16x32_bf16 v[96:99], a[104:107], a[44:47], v[96:99]\n\t"
      "ds_read_b64_tr_b16 a[72:73], v226 offset:32\n\t"
      "v_mfma_f32_16x16x32_bf16 v[92:95], a[100:103], a[44:47], v[92:95]\n\t"
      "ds_read_b64_tr_b16 a[74:75], v226 offset:928\n\t"
      "v_mfma_f32_16x16x32_bf16 v[88:91], a[96:99], a[44:47], v[88:91]\n\t"
      "ds_read_b64_tr_b16 a[76:77], v227 offset:32\n\t"
      "v_mfma_f32_16x16x32_bf16 v[84:87], a[92:95], a[44:47], v[84:87]\n\t"
      "ds_read_b64_tr_b16 a[78:79], v227 offset:928\n\t"
      "buffer_load_dwordx4 v235, %[srd], s41 offen offset:3072 lds\n\t"
      "v_mfma_f32_16x16x32_bf16 v[112:115], a[92:95], a[48:51], v[112:115]\n\t"
      "ds_read_b64_tr_b16 a[80:81], v226 offset:64\n\t"
      "v_mfma_f32_16x16x32_bf16 v[116:119], a[96:99], a[48:51], v[116:119]\n\t"
      "ds_read_b64_tr_b16 a[82:83], v226 offset:960\n\t"
      "v_mfma_f32_16x16x32_bf16 v[120:123], a[100:103], a[48:51], v[120:123]\n\t"
      "ds_read_b64_tr_b16 a[84:85], v227 offset:64\n\t"
      "v_mfma_f32_16x16x32_bf16 v[124:127], a[104:107], a[48:51], v[124:127]\n\t"
      "ds_read_b64_tr_b16 a[86:87], v227 offset:960\n\t"
      "s_add_u32 m0, s43, 11264\n\t"
      "v_mfma_f32_16x16x32_bf16 v[128:131], a[108:111], a[48:51], v[128:131]\n\t"
      "ds_read_b64_tr_b16 a[88:89], v228 offset:0\n\t"
      "buffer_load_dwordx4 v236, %[srd], s41 offen offset:0 lds\n\t"
      "v_mfma_f32_16x16x32_bf16 v[132:135], a[112:115], a[48:51], v[132:135]\n\t"
      "ds_read_b64_tr_b16 a[90:91], v228 offset:896\n\t"
      "v_mfma_f32_16x16x32_bf16 v[136:139], a[116:119], a[48:51], v[136:139]\n\t"
      "ds_read_b128 a[0:3], v224 offset:0\n\t"
      "v_mfma_f32_16x16x32_bf16 v[164:167], a[116:119], a[52:55], v[164:167]\n\t"
      "ds_read_b128 a[4:7], v224 offset:2048\n\t"
      "v_mfma_f32_16x16x32_bf16 v[160:163], a[112:115], a[52:55], v[160:163]\n\t"
      "ds_read_b128 a[8:11], v224 offset:4096\n\t"
      "v_mfma_f32_16x16x32_bf16 v[156:159], a[108:111], a[52:55], v[156:159]\n\t"
      "ds_read_b128 a[12:15], v224 offset:6144\n\t"
      "buffer_load_dwordx4 v237, %[srd], s41 offen offset:1024 lds\n\t"
      "v_mfma_f32_16x16x32_bf16 v[152:155], a[104:107], a[52:55], v[152:155]\n\t"
      "ds_read_b128 a[16:19], v224 offset:8192\n\t"
      "v_mfma_f32_16x16x32_bf16 v[148:151], a[100:103], a[52:55], v[148:151]\n\t"
      "ds_read_b128 a[20:23], v224 offset:10240\n\t"
      "v_mfma_f32_16x16x32_bf16 v[144:147], a[96:99], a[52:55], v[144:147]\n\t"
      "ds_read_b128 a[24:27], v224 offset:12288\n\t"
      "v_mfma_f32_16x16x32_bf16 v[140:143], a[92:95], a[52:55], v[140:143]\n\t"
      "ds_read_b128 a[28:31], v224 offset:14336\n\t"
      "v_mfma_f32_16x16x32_bf16 v[168:171], a[92:95], a[56:59], v[168:171]\n\t"
      "buffer_load_dwordx4 v238, %[srd], s41 offen offset:2048 lds\n\t"
      "v_mfma_f32_16x16x32_bf16 v[172:175], a[96:99], a[56:59], v[172:175]\n\t"
      "s_add_u32 s40, s40, 458752\n\t"
      "v_mfma_f32_16x16x32_bf16 v[176:179], a[100:103], a[56:59], v[176:179]\n\t"
      "s_add_u32 s41, s41, 458752\n\t"
      "v_mfma_f32_16x16x32_bf16 v[180:183], a[104:107], a[56:59], v[180:183]\n\t"
      "s_add_u32 s43, s43, s46\n\t"
      "v_mfma_f32_16x16x32_bf16 v[184:187], a[108:111], a[56:59], v[184:187]\n\t"
      "v_mfma_f32_16x16x32_bf16 v[188:191], a[112:115], a[56:59], v[188:191]\n\t"
      "global_load_dword v248, v[250:251], off\n\t"
      "v_mfma_f32_16x16x32_bf16 v[192:195], a[116:119], a[56:59], v[192:195]\n\t"
      "v_add_co_u32 v250, vcc, v249, v250\n\t"
      "v_mfma_f32_16x16x32_bf16 v[220:223], a[116:119], a[60:63], v[220:223]\n\t"
      "v_mfma_f32_16x16x32_bf16 v[216:219], a[112:115], a[60:63], v[216:219]\n\t"
      "v_addc_co_u32 v251, vcc, 0, v251, vcc\n\t"
      "v_mfma_f32_16x16x32_bf16 v[212:215], a[108:111], a[60:63], v[212:215]\n\t"
      "v_mfma_f32_16x16x32_bf16 v[208:211], a[104:107], a[60:63], v[208:211]\n\t"
      "s_sub_u32 s47, 0, s47\n\t"
      "v_mfma_f32_16x16x32_bf16 v[204:207], a[100:103], a[60:63], v[204:207]\n\t"
      "v_mfma_f32_16x16x32_bf16 v[200:203], a[96:99], a[60:63], v[200:203]\n\t"
      "s_waitcnt lgkmcnt(0)\n\t"
      "v_mfma_f32_16x16x32_bf16 v[196:199], a[92:95], a[60:63], v[196:199]\n\t"
      "v_mfma_f32_16x16x32_bf16 v[0:3], a[64:67], a[0:3], v[0:3]\n\t"
      "ds_read_b64_tr_b16 a[92:93], v226 offset:7168\n\t"
      "v_mfma_f32_16x16x32_bf16 v[4:7], a[68:71], a[0:3], v[4:7]\n\t"
      "ds_read_b64_tr_b16 a[94:95], v226 offset:8064\n\t"
      "v_mfma_f32_16x16x32_bf16 v[8:11], a[72:75], a[0:3], v[8:11]\n\t"
      "ds_read_b64_tr_b16 a[96:97], v227 offset:7168\n\t"
      "v_mfma_f32_16x16x32_bf16 v[12:15], a[76:79], a[0:3], v[12:15]\n\t"
      "ds_read_b64_tr_b16 a[98:99], v227 offset:8064\n\t"
      "v_mfma_f32_16x16x32_bf16 v[16:19], a[80:83], a[0:3], v[16:19]\n\t"
      "ds_read_b64_tr_b16 a[100:101], v226 offset:7200\n\t"
      "v_mfma_f32_16x16x32_bf16 v[20:23], a[84:87], a[0:3], v[20:23]\n\t"
      "ds_read_b64_tr_b16 a[102:103], v226 offset:8096\n\t"
      "v_mfma_f32_16x16x32_bf16 v[24:27], a[88:91], a[0:3], v[24:27]\n\t"
      "ds_read_b64_tr_b16 a[104:105], v227 offset:7200\n\t"
      "v_mfma_f32_16x16x32_bf16 v[52:55], a[88:91], a[4:7], v[52:55]\n\t"
      "ds_read_b64_tr_b16 a[106:107], v227 offset:8096\n\t"
      "v_mfma_f32_16x16x32_bf16 v[48:51], a[84:87], a[4:7], v[48:51]\n\t"
      "ds_read_b64_tr_b16 a[108:109], v226 offset:7232\n\t"
      "v_mfma_f32_16x16x32_bf16 v[44:47], a[80:83], a[4:7], v[44:47]\n\t"
      "ds_read_b64_tr_b16 a[110:111], v226 offset:8128\n\t"
      "v_mfma_f32_16x16x32_bf16 v[40:43], a[76:79], a[4:7], v[40:43]\n\t"
      "ds_read_b64_tr_b16 a[112:113], v227 offset:7232\n\t"
      "v_mfma_f32_16x16x32_bf16 v[36:39], a[72:75], a[4:7], v[36:39]\n\t"
      "ds_read_b64_tr_b16 a[114:115], v227 offset:8128\n\t"
      "v_mfma_f32_16x16x32_bf16 v[32:35], a[68:71], a[4:7], v[32:35]\n\t"
      "ds_read_b64_tr_b16 a[116:117], v228 offset:7168\n\t"
      "v_mfma_f32_16x16x32_bf16 v[28:31], a[64:67], a[4:7], v[28:31]\n\t"
      "ds_read_b64_tr_b16 a[118:119], v228 offset:8064\n\t"
      "v_mfma_f32_16x16x32_bf16 v[56:59], a[64:67], a[8:11], v[56:59]\n\t"
      "ds_read_b128 a[32:35], v225 offset:0\n\t"
      "v_mfma_f32_16x16x32_bf16 v[60:63], a[68:71], a[8:11], v[60:63]\n\t"
      "ds_read_b128 a[36:39], v225 offset:2048\n\t"
      "v_mfma_f32_16x16x32_bf16 v[64:67], a[72:75], a[8:11], v[64:67]\n\t"
      "ds_read_b128 a[40:43], v225 offset:4096\n\t"
      "v_mfma_f32_16x16x32_bf16 v[68:71], a[76:79], a[8:11], v[68:71]\n\t"
      "ds_read_b128 a[44:47], v225 offset:6144\n\t"
      "v_mfma_f32_16x16x32_bf16 v[72:75], a[80:83], a[8:11], v[72:75]\n\t"
      "ds_read_b128 a[48:51], v225 offset:8192\n\t"
      "v_mfma_f32_16x16x32_bf16 v[76:79], a[84:87], a[8:11], v[76:79]\n\t"
      "ds_read_b128 a[52:55], v225 offset:10240\n\t"
      "v_mfma_f32_16x16x32_bf16 v[80:83], a[88:91], a[8:11], v[80:83]\n\t"
      "ds_read_b128 a[56:59], v225 offset:12288\n\t"
      "v_mfma_f32_16x16x32_bf16 v[108:111], a[88:91], a[12:15], v[108:111]\n\t"
      "ds_read_b128 a[60:63], v225 offset:14336\n\t"
      "v_mfma_f32_16x16x32_bf16 v[104:107], a[84:87], a[12:15], v[104:107]\n\t"
      "s_add_u32 s44, s44, 1\n\t"
      "v_mfma_f32_16x16x32_bf16 v[100:103], a[80:83], a[12:15], v[100:103]\n\t"
      "s_cmp_eq_u32 s44, 3\n\t"
      "s_cselect_b32 s46, s49, s48\n\t"
      "s_cselect_b32 s44, 0, s44\n\t"
      "v_mfma_f32_16x16x32_bf16 v[96:99], a[76:79], a[12:15], v[96:99]\n\t"
      "v_add_u32 v224, s47, v224\n\t"
      "v_mfma_f32_16x16x32_bf16 v[92:95], a[72:75], a[12:15], v[92:95]\n\t"
      "v_add_u32 v225, s47, v225\n\t"
      "v_mfma_f32_16x16x32_bf16 v[88:91], a[68:71], a[12:15], v[88:91]\n\t"
      "v_add_u32 v226, s46, v226\n\t"
      "v_mfma_f32_16x16x32_bf16 v[84:87], a[64:67], a[12:15], v[84:87]\n\t"
      "v_add_u32 v227, s46, v227\n\t"
      "v_mfma_f32_16x16x32_bf16 v[112:115], a[64:67], a[16:19], v[112:115]\n\t"
      "v_add_u32 v228, s46, v228\n\t"
      "v_mfma_f32_16x16x32_bf16 v[116:119], a[68:71], a[16:19], v[116:119]\n\t"
      "v_mfma_f32_16x16x32_bf16 v[120:123], a[72:75], a[16:19], v[120:123]\n\t"
      "s_waitcnt vmcnt(15) lgkmcnt(0)\n\t"
      "s_barrier\n\t"
      "v_mfma_f32_16x16x32_bf16 v[124:127], a[76:79], a[16:19], v[124:127]\n\t"
      "v_mfma_f32_16x16x32_bf16 v[128:131], a[80:83], a[16:19], v[128:131]\n\t"
      "s_add_u32 m0, s43, 0\n\t"
      "v_mfma_f32_16x16x32_bf16 v[132:135], a[84:87], a[16:19], v[132:135]\n\t"
      "buffer_load_dwordx4 v232, %[srd], s40 offen offset:0 lds\n\t"
      "v_mfma_f32_16x16x32_bf16 v[136:139], a[88:91], a[16:19], v[136:139]\n\t"
      "v_mfma_f32_16x16x32_bf16 v[164:167], a[88:91], a[20:23], v[164:167]\n\t"
      "v_mfma_f32_16x16x32_bf16 v[160:163], a[84:87], a[20:23], v[160:163]\n\t"
      "v_mfma_f32_16x16x32_bf16 v[156:159], a[80:83], a[20:23], v[156:159]\n\t"
      "v_mfma_f32_16x16x32_bf16 v[152:155], a[76:79], a[20:23], v[152:155]\n\t"
      "buffer_load_dwordx4 v233, %[srd], s40 offen offset:1024 lds\n\t"
      "v_mfma_f32_16x16x32_bf16 v[148:151], a[72:75], a[20:23], v[148:151]\n\t"
      "v_mfma_f32_16x16x32_bf16 v[144:147], a[68:71], a[20:23], v[144:147]\n\t"
      "v_mfma_f32_16x16x32_bf16 v[140:143], a[64:67], a[20:23], v[140:143]\n\t"
      "v_mfma_f32_16x16x32_bf16 v[168:171], a[64:67], a[24:27], v[168:171]\n\t"
      "v_mfma_f32_16x16x32_bf16 v[172:175], a[68:71], a[24:27], v[172:175]\n\t"
      "buffer_load_dwordx4 v234, %[srd], s40 offen offset:2048 lds\n\t"
      "v_mfma_f32_16x16x32_bf16 v[176:179], a[72:75], a[24:27], v[176:179]\n\t"
      "v_mfma_f32_16x16x32_bf16 v[180:183], a[76:79], a[24:27], v[180:183]\n\t"
      "v_mfma_f32_16x16x32_bf16 v[184:187], a[80:83], a[24:27], v[184:187]\n\t"
      "v_mfma_f32_16x16x32_bf16 v[188:191], a[84:87], a[24:27], v[188:191]\n\t"
      "v_mfma_f32_16x16x32_bf16 v[192:195], a[88:91], a[24:27], v[192:195]\n\t"
      "buffer_load_dwordx4 v235, %[srd], s40 offen offset:3072 lds\n\t"
      "v_mfma_f32_16x16x32_bf16 v[220:223], a[88:91], a[28:31], v[220:223]\n\t"
      "v_mfma_f32_16x16x32_bf16 v[216:219], a[84:87], a[28:31], v[216:219]\n\t"
      "v_mfma_f32_16x16x32_bf16 v[212:215], a[80:83], a[28:31], v[212:215]\n\t"
      "v_mfma_f32_16x16x32_bf16 v[208:211], a[76:79], a[28:31], v[208:211]\n\t"
      "s_add_u32 m0, s43, 4096\n\t"
      "v_mfma_f32_16x16x32_bf16 v[204:207], a[72:75], a[28:31], v[204:207]\n\t"
      "buffer_load_dwordx4 v236, %[srd], s40 offen offset:0 lds\n\t"
      "v_mfma_f32_16x16x32_bf16 v[200:203], a[68:71], a[28:31], v[200:203]\n\t"
      "v_mfma_f32_16x16x32_bf16 v[196:199], a[64:67], a[28:31], v[196:199]\n\t"
      "v_mfma_f32_16x16x32_bf16 v[0:3], a[92:95], a[32:35], v[0:3]\n\t"
      "v_mfma_f32_16x16x32_bf16 v[4:7], a[96:99], a[32:35], v[4:7]\n\t"
      "v_mfma_f32_16x16x32_bf16 v[8:11], a[100:103], a[32:35], v[8:11]\n\t"
      "buffer_load_dwordx4 v237, %[srd], s40 offen offset:1024 lds\n\t"
      "v_mfma_f32_16x16x32_bf16 v[12:15], a[104:107], a[32:35], v[12:15]\n\t"
      "v_mfma_f32_16x16x32_bf16 v[16:19], a[108:111], a[32:35], v[16:19]\n\t"
      "v_mfma_f32_16x16x32_bf16 v[20:23], a[112:115], a[32:35], v[20:23]\n\t"
      "v_mfma_f32_16x16x32_bf16 v[24:27], a[116:119], a[32:35], v[24:27]\n\t"
      "v_mfma_f32_16x16x32_bf16 v[52:55], a[116:119], a[36:39], v[52:55]\n\t"
      "buffer_load_dwordx4 v238, %[srd], s40 offen offset:2048 lds\n\t"
      "v_mfma_f32_16x16x32_bf16 v[48:51], a[112:115], a[36:39], v[48:51]\n\t"
      "v_mfma_f32_16x16x32_bf16 v[44:47], a[108:111], a[36:39], v[44:47]\n\t"
      "v_mfma_f32_16x16x32_bf16 v[40:43], a[104:107], a[36:39], v[40:43]\n\t"
      "v_mfma_f32_16x16x32_bf16 v[36:39], a[100:103], a[36:39], v[36:39]\n\t"
      "s_add_u32 m0, s43, 7168\n\t"
      "v_mfma_f32_16x16x32_bf16 v[32:35], a[96:99], a[36:39], v[32:35]\n\t"
      "buffer_load_dwordx4 v232, %[srd], s41 offen offset:0 lds\n\t"
      "v_mfma_f32_16x16x32_bf16 v[28:31], a[92:95], a[36:39], v[28:31]\n\t"
      "v_mfma_f32_16x16x32_bf16 v[56:59], a[92:95], a[40:43], v[56:59]\n\t"
      "v_mfma_f32_16x16x32_bf16 v[60:63], a[96:99], a[40:43], v[60:63]\n\t"
      "v_mfma_f32_16x16x32_bf16 v[64:67], a[100:103], a[40:43], v[64:67]\n\t"
      "v_mfma_f32_16x16x32_bf16 v[68:71], a[104:107], a[40:43], v[68:71]\n\t"
      "buffer_load_dwordx4 v233, %[srd], s41 offen offset:1024 lds\n\t"
      "v_mfma_f32_16x16x32_bf16 v[72:75], a[108:111], a[40:43], v[72:75]\n\t"
      "v_mfma_f32_16x16x32_bf16 v[76:79], a[112:115], a[40:43], v[76:79]\n\t"
      "v_mfma_f32_16x16x32_bf16 v[80:83], a[116:119], a[40:43], v[80:83]\n\t"
      "ds_read_b64_tr_b16 a[64:65], v226 offset:0\n\t"
      "v_mfma_f32_16x16x32_bf16 v[108:111], a[116:119], a[44:47], v[108:111]\n\t"
      "ds_read_b64_tr_b16 a[66:67], v226 offset:896\n\t"
      "v_mfma_f32_16x16x32_bf16 v[104:107], a[112:115], a[44:47], v[104:107]\n\t"
      "ds_read_b64_tr_b16 a[68:69], v227 offset:0\n\t"
      "buffer_load_dwordx4 v234, %[srd], s41 offen offset:2048 lds\n\t"
      "v_mfma_f32_16x16x32_bf16 v[100:103], a[108:111], a[44:47], v[100:103]\n\t"
      "ds_read_b64_tr_b16 a[70:71], v227 offset:896\n\t"
      "v_mfma_f32_16x16x32_bf16 v[96:99], a[104:107], a[44:47], v[96:99]\n\t"
      "ds_read_b64_tr_b16 a[72:73], v226 offset:32\n\t"
      "v_mfma_f32_16x16x32_bf16 v[92:95], a[100:103], a[44:47], v[92:95]\n\t"
      "ds_read_b64_tr_b16 a[74:75], v226 offset:928\n\t"
      "v_mfma_f32_16x16x32_bf16 v[88:91], a[96:99], a[44:47], v[88:91]\n\t"
      "ds_read_b64_tr_b16 a[76:77], v227 offset:32\n\t"
      "v_mfma_f32_16x16x32_bf16 v[84:87], a[92:95], a[44:47], v[84:87]\n\t"
      "ds_read_b64_tr_b16 a[78:79], v227 offset:928\n\t"
      "buffer_load_dwordx4 v235, %[srd], s41 offen offset:3072 lds\n\t"
      "v_mfma_f32_16x16x32_bf16 v[112:115], a[92:95], a[48:51], v[112:115]\n\t"
      "ds_read_b64_tr_b16 a[80:81], v226 offset:64\n\t"
      "v_mfma_f32_16x16x32_bf16 v[116:119], a[96:99], a[48:51], v[116:119]\n\t"
      "ds_read_b64_tr_b16 a[82:83], v226 offset:960\n\t"
      "v_mfma_f32_16x16x32_bf16 v[120:123], a[100:103], a[48:51], v[120:123]\n\t"
      "ds_read_b64_tr_b16 a[84:85], v227 offset:64\n\t"
      "v_mfma_f32_16x16x32_bf16 v[124:127], a[104:107], a[48:51], v[124:127]\n\t"
      "ds_read_b64_tr_b16 a[86:87], v227 offset:960\n\t"
      "s_add_u32 m0, s43, 11264\n\t"
      "v_mfma_f32_16x16x32_bf16 v[128:131], a[108:111], a[48:51], v[128:131]\n\t"
      "ds_read_b64_tr_b16 a[88:89], v228 offset:0\n\t"
      "buffer_load_dwordx4 v236, %[srd], s41 offen offset:0 lds\n\t"
      "v_mfma_f32_16x16x32_bf16 v[132:135], a[112:115], a[48:51], v[132:135]\n\t"
      "ds_read_b64_tr_b16 a[90:91], v228 offset:896\n\t"
      "v_mfma_f32_16x16x32_bf16 v[136:139], a[116:119], a[48:51], v[136:139]\n\t"
      "ds_read_b128 a[0:3], v224 offset:0\n\t"
      "v_mfma_f32_16x16x32_bf16 v[164:167], a[116:119], a[52:55], v[164:167]\n\t"
      "ds_read_b128 a[4:7], v224 offset:2048\n\t"
      "v_mfma_f32_16x16x32_bf16 v[160:163], a[112:115], a[52:55], v[160:163]\n\t"
      "ds_read_b128 a[8:11], v224 offset:4096\n\t"
      "v_mfma_f32_16x16x32_bf16 v[156:159], a[108:111], a[52:55], v[156:159]\n\t"
      "ds_read_b128 a[12:15], v224 offset:6144\n\t"
      "buffer_load_dwordx4 v237, %[srd], s41 offen offset:1024 lds\n\t"
      "v_mfma_f32_16x16x32_bf16 v[152:155], a[104:107], a[52:55], v[152:155]\n\t"
      "ds_read_b128 a[16:19], v224 offset:8192\n\t"
      "v_mfma_f32_16x16x32_bf16 v[148:151], a[100:103], a[52:55], v[148:151]\n\t"
      "ds_read_b128 a[20:23], v224 offset:10240\n\t"
      "v_mfma_f32_16x16x32_bf16 v[144:147], a[96:99], a[52:55], v[144:147]\n\t"
      "ds_read_b128 a[24:27], v224 offset:12288\n\t"
      "v_mfma_f32_16x16x32_bf16 v[140:143], a[92:95], a[52:55], v[140:143]\n\t"
      "ds_read_b128 a[28:31], v224 offset:14336\n\t"
      "v_mfma_f32_16x16x32_bf16 v[168:171], a[92:95], a[56:59], v[168:171]\n\t"
      "buffer_load_dwordx4 v238, %[srd], s41 offen offset:2048 lds\n\t"
      "v_mfma_f32_16x16x32_bf16 v[172:175], a[96:99], a[56:59], v[172:175]\n\t"
      "s_add_u32 s40, s40, 458752\n\t"
      "v_mfma_f32_16x16x32_bf16 v[176:179], a[100:103], a[56:59], v[176:179]\n\t"
      "s_add_u32 s41, s41, 458752\n\t"
      "v_mfma_f32_16x16x32_bf16 v[180:183], a[104:107], a[56:59], v[180:183]\n\t"
      "s_add_u32 s43, s43, s46\n\t"
      "v_mfma_f32_16x16x32_bf16 v[184:187], a[108:111], a[56:59], v[184:187]\n\t"
      "v_mfma_f32_16x16x32_bf16 v[188:191], a[112:115], a[56:59], v[188:191]\n\t"
      "global_load_dword v248, v[250:251], off\n\t"
      "v_mfma_f32_16x16x32_bf16 v[192:195], a[116:119], a[56:59], v[192:195]\n\t"
      "v_add_co_u32 v250, vcc, v249, v250\n\t"
      "v_mfma_f32_16x16x32_bf16 v[220:223], a[116:119], a[60:63], v[220:223]\n\t"
      "v_mfma_f32_16x16x32_bf16 v[216:219], a[112:115], a[60:63], v[216:219]\n\t"
      "v_addc_co_u32 v251, vcc, 0, v251, vcc\n\t"
      "v_mfma_f32_16x16x32_bf16 v[212:215], a[108:111], a[60:63], v[212:215]\n\t"
      "v_mfma_f32_16x16x32_bf16 v[208:211], a[104:107], a[60:63], v[208:211]\n\t"
      "s_sub_u32 s47, 0, s47\n\t"
      "v_mfma_f32_16x16x32_bf16 v[204:207], a[100:103], a[60:63], v[204:207]\n\t"
      "v_mfma_f32_16x16x32_bf16 v[200:203], a[96:99], a[60:63], v[200:203]\n\t"
      "s_waitcnt lgkmcnt(0)\n\t"
      "v_mfma_f32_16x16x32_bf16 v[196:199], a[92:95], a[60:63], v[196:199]\n\t"
      "s_mov_b32 s45, 9\n\t"
      "3:\n\t"
      "v_mfma_f32_16x16x32_bf16 v[0:3], a[64:67], a[0:3], v[0:3]\n\t"
      "ds_read_b64_tr_b16 a[92:93], v226 offset:7168\n\t"
      "v_mfma_f32_16x16x32_bf16 v[4:7], a[68:71], a[0:3], v[4:7]\n\t"
      "ds_read_b64_tr_b16 a[94:95], v226 offset:8064\n\t"
      "v_mfma_f32_16x16x32_bf16 v[8:11], a[72:75], a[0:3], v[8:11]\n\t"
      "ds_read_b64_tr_b16 a[96:97], v227 offset:7168\n\t"
      "v_mfma_f32_16x16x32_bf16 v[12:15], a[76:79], a[0:3], v[12:15]\n\t"
      "ds_read_b64_tr_b16 a[98:99], v227 offset:8064\n\t"
      "v_mfma_f32_16x16x32_bf16 v[16:19], a[80:83], a[0:3], v[16:19]\n\t"
      "ds_read_b64_tr_b16 a[100:101], v226 offset:7200\n\t"
      "v_mfma_f32_16x16x32_bf16 v[20:23], a[84:87], a[0:3], v[20:23]\n\t"
      "ds_read_b64_tr_b16 a[102:103], v226 offset:8096\n\t"
      "v_mfma_f32_16x16x32_bf16 v[24:27], a[88:91], a[0:3], v[24:27]\n\t"
      "ds_read_b64_tr_b16 a[104:105], v227 offset:7200\n\t"
      "v_mfma_f32_16x16x32_bf16 v[52:55], a[88:91], a[4:7], v[52:55]\n\t"
      "ds_read_b64_tr_b16 a[106:107], v227 offset:8096\n\t"
      "v_mfma_f32_16x16x32_bf16 v[48:51], a[84:87], a[4:7], v[48:51]\n\t"
      "ds_read_b64_tr_b16 a[108:109], v226 offset:7232\n\t"
      "v_mfma_f32_16x16x32_bf16 v[44:47], a[80:83], a[4:7], v[44:47]\n\t"
      "ds_read_b64_tr_b16 a[110:111], v226 offset:8128\n\t"
      "v_mfma_f32_16x16x32_bf16 v[40:43], a[76:79], a[4:7], v[40:43]\n\t"
      "ds_read_b64_tr_b16 a[112:113], v227 offset:7232\n\t"
      "v_mfma_f32_16x16x32_bf16 v[36:39], a[72:75], a[4:7], v[36:39]\n\t"
      "ds_read_b64_tr_b16 a[114:115], v227 offset:8128\n\t"
      "v_mfma_f32_16x16x32_bf16 v[32:35], a[68:71], a[4:7], v[32:35]\n\t"
      "ds_read_b64_tr_b16 a[116:117], v228 offset:7168\n\t"
      "v_mfma_f32_16x16x32_bf16 v[28:31], a[64:67], a[4:7], v[28:31]\n\t"
      "ds_read_b64_tr_b16 a[118:119], v228 offset:8064\n\t"
      "v_mfma_f32_16x16x32_bf16 v[56:59], a[64:67], a[8:11], v[56:59]\n\t"
      "ds_read_b128 a[32:35], v225 offset:0\n\t"
      "v_mfma_f32_16x16x32_bf16 v[60:63], a[68:71], a[8:11], v[60:63]\n\t"
      "ds_read_b128 a[36:39], v225 offset:2048\n\t"
      "v_mfma_f32_16x16x32_bf16 v[64:67], a[72:75], a[8:11], v[64:67]\n\t"
      "ds_read_b128 a[40:43], v225 offset:4096\n\t"
      "v_mfma_f32_16x16x32_bf16 v[68:71], a[76:79], a[8:11], v[68:71]\n\t"
      "ds_read_b128 a[44:47], v225 offset:6144\n\t"
      "v_mfma_f32_16x16x32_bf16 v[72:75], a[80:83], a[8:11], v[72:75]\n\t"
      "ds_read_b128 a[48:51], v225 offset:8192\n\t"
      "v_mfma_f32_16x16x32_bf16 v[76:79], a[84:87], a[8:11], v[76:79]\n\t"
      "ds_read_b128 a[52:55], v225 offset:10240\n\t"
      "v_mfma_f32_16x16x32_bf16 v[80:83], a[88:91], a[8:11], v[80:83]\n\t"
      "ds_read_b128 a[56:59], v225 offset:12288\n\t"
      "v_mfma_f32_16x16x32_bf16 v[108:111], a[88:91], a[12:15], v[108:111]\n\t"
      "ds_read_b128 a[60:63], v225 offset:14336\n\t"
      "v_mfma_f32_16x16x32_bf16 v[104:107], a[84:87], a[12:15], v[104:107]\n\t"
      "s_add_u32 s44, s44, 1\n\t"
      "v_mfma_f32_16x16x32_bf16 v[100:103], a[80:83], a[12:15], v[100:103]\n\t"
      "s_cmp_eq_u32 s44, 3\n\t"
      "s_cselect_b32 s46, s49, s48\n\t"
      "s_cselect_b32 s44, 0, s44\n\t"
      "v_mfma_f32_16x16x32_bf16 v[96:99], a[76:79], a[12:15], v[96:99]\n\t"
      "v_add_u32 v224, s47, v224\n\t"
      "v_mfma_f32_16x16x32_bf16 v[92:95], a[72:75], a[12:15], v[92:95]\n\t"
      "v_add_u32 v225, s47, v225\n\t"
      "v_mfma_f32_16x16x32_bf16 v[88:91], a[68:71], a[12:15], v[88:91]\n\t"
      "v_add_u32 v226, s46, v226\n\t"
      "v_mfma_f32_16x16x32_bf16 v[84:87], a[64:67], a[12:15], v[84:87]\n\t"
      "v_add_u32 v227, s46, v227\n\t"
      "v_mfma_f32_16x16x32_bf16 v[112:115], a[64:67], a[16:19], v[112:115]\n\t"
      "v_add_u32 v228, s46, v228\n\t"
      "v_mfma_f32_16x16x32_bf16 v[116:119], a[68:71], a[16:19], v[116:119]\n\t"
      "v_mfma_f32_16x16x32_bf16 v[120:123], a[72:75], a[16:19], v[120:123]\n\t"
      "s_waitcnt vmcnt(16) lgkmcnt(0)\n\t"
      "s_barrier\n\t"
      "v_mfma_f32_16x16x32_bf16 v[124:127], a[76:79], a[16:19], v[124:127]\n\t"
      "v_mfma_f32_16x16x32_bf16 v[128:131], a[80:83], a[16:19], v[128:131]\n\t"
      "s_add_u32 m0, s43, 0\n\t"
      "v_mfma_f32_16x16x32_bf16 v[132:135], a[84:87], a[16:19], v[132:135]\n\t"
      "buffer_load_dwordx4 v232, %[srd], s40 offen offset:0 lds\n\t"
      "v_mfma_f32_16x16x32_bf16 v[136:139], a[88:91], a[16:19], v[136:139]\n\t"
      "v_mfma_f32_16x16x32_bf16 v[164:167], a[88:91], a[20:23], v[164:167]\n\t"
      "v_mfma_f32_16x16x32_bf16 v[160:163], a[84:87], a[20:23], v[160:163]\n\t"
      "v_mfma_f32_16x16x32_bf16 v[156:159], a[80:83], a[20:23], v[156:159]\n\t"
      "v_mfma_f32_16x16x32_bf16 v[152:155], a[76:79], a[20:23], v[152:155]\n\t"
      "buffer_load_dwordx4 v233, %[srd], s40 offen offset:1024 lds\n\t"
      "v_mfma_f32_16x16x32_bf16 v[148:151], a[72:75], a[20:23], v[148:151]\n\t"
      "v_mfma_f32_16x16x32_bf16 v[144:147], a[68:71], a[20:23], v[144:147]\n\t"
      "v_mfma_f32_16x16x32_bf16 v[140:143], a[64:67], a[20:23], v[140:143]\n\t"
      "v_mfma_f32_16x16x32_bf16 v[168:171], a[64:67], a[24:27], v[168:171]\n\t"
      "v_mfma_f32_16x16x32_bf16 v[172:175], a[68:71], a[24:27], v[172:175]\n\t"
      "buffer_load_dwordx4 v234, %[srd], s40 offen offset:2048 lds\n\t"
      "v_mfma_f32_16x16x32_bf16 v[176:179], a[72:75], a[24:27], v[176:179]\n\t"
      "v_mfma_f32_16x16x32_bf16 v[180:183], a[76:79], a[24:27], v[180:183]\n\t"
      "v_mfma_f32_16x16x32_bf16 v[184:187], a[80:83], a[24:27], v[184:187]\n\t"
      "v_mfma_f32_16x16x32_bf16 v[188:191], a[84:87], a[24:27], v[188:191]\n\t"
      "v_mfma_f32_16x16x32_bf16 v[192:195], a[88:91], a[24:27], v[192:195]\n\t"
      "buffer_load_dwordx4 v235, %[srd], s40 offen offset:3072 lds\n\t"
      "v_mfma_f32_16x16x32_bf16 v[220:223], a[88:91], a[28:31], v[220:223]\n\t"
      "v_mfma_f32_16x16x32_bf16 v[216:219], a[84:87], a[28:31], v[216:219]\n\t"
      "v_mfma_f32_16x16x32_bf16 v[212:215], a[80:83], a[28:31], v[212:215]\n\t"
      "v_mfma_f32_16x16x32_bf16 v[208:211], a[76:79], a[28:31], v[208:211]\n\t"
      "s_add_u32 m0, s43, 4096\n\t"
      "v_mfma_f32_16x16x32_bf16 v[204:207], a[72:75], a[28:31], v[204:207]\n\t"
      "buffer_load_dwordx4 v236, %[srd], s40 offen offset:0 lds\n\t"
      "v_mfma_f32_16x16x32_bf16 v[200:203], a[68:71], a[28:31], v[200:203]\n\t"
      "v_mfma_f32_16x16x32_bf16 v[196:199], a[64:67], a[28:31], v[196:199]\n\t"
      "v_mfma_f32_16x16x32_bf16 v[0:3], a[92:95], a[32:35], v[0:3]\n\t"
      "v_mfma_f32_16x16x32_bf16 v[4:7], a[96:99], a[32:35], v[4:7]\n\t"
      "v_mfma_f32_16x16x32_bf16 v[8:11], a[100:103], a[32:35], v[8:11]\n\t"
      "buffer_load_dwordx4 v237, %[srd], s40 offen offset:1024 lds\n\t"
      "v_mfma_f32_16x16x32_bf16 v[12:15], a[104:107], a[32:35], v[12:15]\n\t"
      "v_mfma_f32_16x16x32_bf16 v[16:19], a[108:111], a[32:35], v[16:19]\n\t"
      "v_mfma_f32_16x16x32_bf16 v[20:23], a[112:115], a[32:35], v[20:23]\n\t"
      "v_mfma_f32_16x16x32_bf16 v[24:27], a[116:119], a[32:35], v[24:27]\n\t"
      "v_mfma_f32_16x16x32_bf16 v[52:55], a[116:119], a[36:39], v[52:55]\n\t"
      "buffer_load_dwordx4 v238, %[srd], s40 offen offset:2048 lds\n\t"
      "v_mfma_f32_16x16x32_bf16 v[48:51], a[112:115], a[36:39], v[48:51]\n\t"
      "v_mfma_f32_16x16x32_bf16 v[44:47], a[108:111], a[36:39], v[44:47]\n\t"
      "v_mfma_f32_16x16x32_bf16 v[40:43], a[104:107], a[36:39], v[40:43]\n\t"
      "v_mfma_f32_16x16x32_bf16 v[36:39], a[100:103], a[36:39], v[36:39]\n\t"
      "s_add_u32 m0, s43, 7168\n\t"
      "v_mfma_f32_16x16x32_bf16 v[32:35], a[96:99], a[36:39], v[32:35]\n\t"
      "buffer_load_dwordx4 v232, %[srd], s41 offen offset:0 lds\n\t"
      "v_mfma_f32_16x16x32_bf16 v[28:31], a[92:95], a[36:39], v[28:31]\n\t"
      "v_mfma_f32_16x16x32_bf16 v[56:59], a[92:95], a[40:43], v[56:59]\n\t"
      "v_mfma_f32_16x16x32_bf16 v[60:63], a[96:99], a[40:43], v[60:63]\n\t"
      "v_mfma_f32_16x16x32_bf16 v[64:67], a[100:103], a[40:43], v[64:67]\n\t"
      "v_mfma_f32_16x16x32_bf16 v[68:71], a[104:107], a[40:43], v[68:71]\n\t"
      "buffer_load_dwordx4 v233, %[srd], s41 offen offset:1024 lds\n\t"
      "v_mfma_f32_16x16x32_bf16 v[72:75], a[108:111], a[40:43], v[72:75]\n\t"
      "v_mfma_f32_16x16x32_bf16 v[76:79], a[112:115], a[40:43], v[76:79]\n\t"
      "v_mfma_f32_16x16x32_bf16 v[80:83], a[116:119], a[40:43], v[80:83]\n\t"
      "ds_read_b64_tr_b16 a[64:65], v226 offset:0\n\t"
      "v_mfma_f32_16x16x32_bf16 v[108:111], a[116:119], a[44:47], v[108:111]\n\t"
      "ds_read_b64_tr_b16 a[66:67], v226 offset:896\n\t"
      "v_mfma_f32_16x16x32_bf16 v[104:107], a[112:115], a[44:47], v[104:107]\n\t"
      "ds_read_b64_tr_b16 a[68:69], v227 offset:0\n\t"
      "buffer_load_dwordx4 v234, %[srd], s41 offen offset:2048 lds\n\t"
      "v_mfma_f32_16x16x32_bf16 v[100:103], a[108:111], a[44:47], v[100:103]\n\t"
      "ds_read_b64_tr_b16 a[70:71], v227 offset:896\n\t"
      "v_mfma_f32_16x16x32_bf16 v[96:99], a[104:107], a[44:47], v[96:99]\n\t"
      "ds_read_b64_tr_b16 a[72:73], v226 offset:32\n\t"
      "v_mfma_f32_16x16x32_bf16 v[92:95], a[100:103], a[44:47], v[92:95]\n\t"
      "ds_read_b64_tr_b16 a[74:75], v226 offset:928\n\t"
      "v_mfma_f32_16x16x32_bf16 v[88:91], a[96:99], a[44:47], v[88:91]\n\t"
      "ds_read_b64_tr_b16 a[76:77], v227 offset:32\n\t"
      "v_mfma_f32_16x16x32_bf16 v[84:87], a[92:95], a[44:47], v[84:87]\n\t"
      "ds_read_b64_tr_b16 a[78:79], v227 offset:928\n\t"
      "buffer_load_dwordx4 v235, %[srd], s41 offen offset:3072 lds\n\t"
      "v_mfma_f32_16x16x32_bf16 v[112:115], a[92:95], a[48:51], v[112:115]\n\t"
      "ds_read_b64_tr_b16 a[80:81], v226 offset:64\n\t"
      "v_mfma_f32_16x16x32_bf16 v[116:119], a[96:99], a[48:51], v[116:119]\n\t"
      "ds_read_b64_tr_b16 a[82:83], v226 offset:960\n\t"
      "v_mfma_f32_16x16x32_bf16 v[120:123], a[100:103], a[48:51], v[120:123]\n\t"
      "ds_read_b64_tr_b16 a[84:85], v227 offset:64\n\t"
      "v_mfma_f32_16x16x32_bf16 v[124:127], a[104:107], a[48:51], v[124:127]\n\t"
      "ds_read_b64_tr_b16 a[86:87], v227 offset:960\n\t"
      "s_add_u32 m0, s43, 11264\n\t"
      "v_mfma_f32_16x16x32_bf16 v[128:131], a[108:111], a[48:51], v[128:131]\n\t"
      "ds_read_b64_tr_b16 a[88:89], v228 offset:0\n\t"
      "buffer_load_dwordx4 v236, %[srd], s41 offen offset:0 lds\n\t"
      "v_mfma_f32_16x16x32_bf16 v[132:135], a[112:115], a[48:51], v[132:135]\n\t"
      "ds_read_b64_tr_b16 a[90:91], v228 offset:896\n\t"
      "v_mfma_f32_16x16x32_bf16 v[136:139], a[116:119], a[48:51], v[136:139]\n\t"
      "ds_read_b128 a[0:3], v224 offset:0\n\t"
      "v_mfma_f32_16x16x32_bf16 v[164:167], a[116:119], a[52:55], v[164:167]\n\t"
      "ds_read_b128 a[4:7], v224 offset:2048\n\t"
      "v_mfma_f32_16x16x32_bf16 v[160:163], a[112:115], a[52:55], v[160:163]\n\t"
      "ds_read_b128 a[8:11], v224 offset:4096\n\t"
      "v_mfma_f32_16x16x32_bf16 v[156:159], a[108:111], a[52:55], v[156:159]\n\t"
      "ds_read_b128 a[12:15], v224 offset:6144\n\t"
      "buffer_load_dwordx4 v237, %[srd], s41 offen offset:1024 lds\n\t"
      "v_mfma_f32_16x16x32_bf16 v[152:155], a[104:107], a[52:55], v[152:155]\n\t"
      "ds_read_b128 a[16:19], v224 offset:8192\n\t"
      "v_mfma_f32_16x16x32_bf16 v[148:151], a[100:103], a[52:55], v[148:151]\n\t"
      "ds_read_b128 a[20:23], v224 offset:10240\n\t"
      "v_mfma_f32_16x16x32_bf16 v[144:147], a[96:99], a[52:55], v[144:147]\n\t"
      "ds_read_b128 a[24:27], v224 offset:12288\n\t"
      "v_mfma_f32_16x16x32_bf16 v[140:143], a[92:95], a[52:55], v[140:143]\n\t"
      "ds_read_b128 a[28:31], v224 offset:14336\n\t"
      "v_mfma_f32_16x16x32_bf16 v[168:171], a[92:95], a[56:59], v[168:171]\n\t"
      "buffer_load_dwordx4 v238, %[srd], s41 offen offset:2048 lds\n\t"
      "v_mfma_f32_16x16x32_bf16 v[172:175], a[96:99], a[56:59], v[172:175]\n\t"
      "s_add_u32 s40, s40, 458752\n\t"
      "v_mfma_f32_16x16x32_bf16 v[176:179], a[100:103], a[56:59], v[176:179]\n\t"
      "s_add_u32 s41, s41, 458752\n\t"
      "v_mfma_f32_16x16x32_bf16 v[180:183], a[104:107], a[56:59], v[180:183]\n\t"
      "s_add_u32 s43, s43, s46\n\t"
      "v_mfma_f32_16x16x32_bf16 v[184:187], a[108:111], a[56:59], v[184:187]\n\t"
      "v_mfma_f32_16x16x32_bf16 v[188:191], a[112:115], a[56:59], v[188:191]\n\t"
      "global_load_dword v248, v[250:251], off\n\t"
      "v_mfma_f32_16x16x32_bf16 v[192:195], a[116:119], a[56:59], v[192:195]\n\t"
      "v_add_co_u32 v250, vcc, v249, v250\n\t"
      "v_mfma_f32_16x16x32_bf16 v[220:223], a[116:119], a[60:63], v[220:223]\n\t"
      "v_mfma_f32_16x16x32_bf16 v[216:219], a[112:115], a[60:63], v[216:219]\n\t"
      "v_addc_co_u32 v251, vcc, 0, v251, vcc\n\t"
      "v_mfma_f32_16x16x32_bf16 v[212:215], a[108:111], a[60:63], v[212:215]\n\t"
      "v_mfma_f32_16x16x32_bf16 v[208:211], a[104:107], a[60:63], v[208:211]\n\t"
      "s_sub_u32 s47, 0, s47\n\t"
      "v_mfma_f32_16x16x32_bf16 v[204:207], a[100:103], a[60:63], v[204:207]\n\t"
      "v_mfma_f32_16x16x32_bf16 v[200:203], a[96:99], a[60:63], v[200:203]\n\t"
      "s_waitcnt lgkmcnt(0)\n\t"
      "v_mfma_f32_16x16x32_bf16 v[196:199], a[92:95], a[60:63], v[196:199]\n\t"
      "s_sub_u32 s45, s45, 1\n\t"
      "s_cmp_lg_u32 s45, 0\n\t"
      "s_cbranch_scc1 3b\n\t"
      "v_mfma_f32_16x16x32_bf16 v[0:3], a[64:67], a[0:3], v[0:3]\n\t"
      "ds_read_b64_tr_b16 a[92:93], v226 offset:7168\n\t"
      "v_mfma_f32_16x16x32_bf16 v[4:7], a[68:71], a[0:3], v[4:7]\n\t"
      "ds_read_b64_tr_b16 a[94:95], v226 offset:8064\n\t"
      "v_mfma_f32_16x16x32_bf16 v[8:11], a[72:75], a[0:3], v[8:11]\n\t"
      "ds_read_b64_tr_b16 a[96:97], v227 offset:7168\n\t"
      "v_mfma_f32_16x16x32_bf16 v[12:15], a[76:79], a[0:3], v[12:15]\n\t"
      "ds_read_b64_tr_b16 a[98:99], v227 offset:8064\n\t"
      "v_mfma_f32_16x16x32_bf16 v[16:19], a[80:83], a[0:3], v[16:19]\n\t"
      "ds_read_b64_tr_b16 a[100:101], v226 offset:7200\n\t"
      "v_mfma_f32_16x16x32_bf16 v[20:23], a[84:87], a[0:3], v[20:23]\n\t"
      "ds_read_b64_tr_b16 a[102:103], v226 offset:8096\n\t"
      "v_mfma_f32_16x16x32_bf16 v[24:27], a[88:91], a[0:3], v[24:27]\n\t"
      "ds_read_b64_tr_b16 a[104:105], v227 offset:7200\n\t"
      "v_mfma_f32_16x16x32_bf16 v[52:55], a[88:91], a[4:7], v[52:55]\n\t"
      "ds_read_b64_tr_b16 a[106:107], v227 offset:8096\n\t"
      "v_mfma_f32_16x16x32_bf16 v[48:51], a[84:87], a[4:7], v[48:51]\n\t"
      "ds_read_b64_tr_b16 a[108:109], v226 offset:7232\n\t"
      "v_mfma_f32_16x16x32_bf16 v[44:47], a[80:83], a[4:7], v[44:47]\n\t"
      "ds_read_b64_tr_b16 a[110:111], v226 offset:8128\n\t"
      "v_mfma_f32_16x16x32_bf16 v[40:43], a[76:79], a[4:7], v[40:43]\n\t"
      "ds_read_b64_tr_b16 a[112:113], v227 offset:7232\n\t"
      "v_mfma_f32_16x16x32_bf16 v[36:39], a[72:75], a[4:7], v[36:39]\n\t"
      "ds_read_b64_tr_b16 a[114:115], v227 offset:8128\n\t"
      "v_mfma_f32_16x16x32_bf16 v[32:35], a[68:71], a[4:7], v[32:35]\n\t"
      "ds_read_b64_tr_b16 a[116:117], v228 offset:7168\n\t"
      "v_mfma_f32_16x16x32_bf16 v[28:31], a[64:67], a[4:7], v[28:31]\n\t"
      "ds_read_b64_tr_b16 a[118:119], v228 offset:8064\n\t"
      "v_mfma_f32_16x16x32_bf16 v[56:59], a[64:67], a[8:11], v[56:59]\n\t"
      "ds_read_b128 a[32:35], v225 offset:0\n\t"
      "v_mfma_f32_16x16x32_bf16 v[60:63], a[68:71], a[8:11], v[60:63]\n\t"
      "ds_read_b128 a[36:39], v225 offset:2048\n\t"
      "v_mfma_f32_16x16x32_bf16 v[64:67], a[72:75], a[8:11], v[64:67]\n\t"
      "ds_read_b128 a[40:43], v225 offset:4096\n\t"
      "v_mfma_f32_16x16x32_bf16 v[68:71], a[76:79], a[8:11], v[68:71]\n\t"
      "ds_read_b128 a[44:47], v225 offset:6144\n\t"
      "v_mfma_f32_16x16x32_bf16 v[72:75], a[80:83], a[8:11], v[72:75]\n\t"
      "ds_read_b128 a[48:51], v225 offset:8192\n\t"
      "v_mfma_f32_16x16x32_bf16 v[76:79], a[84:87], a[8:11], v[76:79]\n\t"
      "ds_read_b128 a[52:55], v225 offset:10240\n\t"
      "v_mfma_f32_16x16x32_bf16 v[80:83], a[88:91], a[8:11], v[80:83]\n\t"
      "ds_read_b128 a[56:59], v225 offset:12288\n\t"
      "v_mfma_f32_16x16x32_bf16 v[108:111], a[88:91], a[12:15], v[108:111]\n\t"
      "ds_read_b128 a[60:63], v225 offset:14336\n\t"
      "v_mfma_f32_16x16x32_bf16 v[104:107], a[84:87], a[12:15], v[104:107]\n\t"
      "s_add_u32 s44, s44, 1\n\t"
      "v_mfma_f32_16x16x32_bf16 v[100:103], a[80:83], a[12:15], v[100:103]\n\t"
      "s_cmp_eq_u32 s44, 3\n\t"
      "s_cselect_b32 s46, s49, s48\n\t"
      "s_cselect_b32 s44, 0, s44\n\t"
      "v_mfma_f32_16x16x32_bf16 v[96:99], a[76:79], a[12:15], v[96:99]\n\t"
      "v_add_u32 v224, s47, v224\n\t"
      "v_mfma_f32_16x16x32_bf16 v[92:95], a[72:75], a[12:15], v[92:95]\n\t"
      "v_add_u32 v225, s47, v225\n\t"
      "v_mfma_f32_16x16x32_bf16 v[88:91], a[68:71], a[12:15], v[88:91]\n\t"
      "v_add_u32 v226, s46, v226\n\t"
      "v_mfma_f32_16x16x32_bf16 v[84:87], a[64:67], a[12:15], v[84:87]\n\t"
      "v_add_u32 v227, s46, v227\n\t"
      "v_mfma_f32_16x16x32_bf16 v[112:115], a[64:67], a[16:19], v[112:115]\n\t"
      "v_add_u32 v228, s46, v228\n\t"
      "v_mfma_f32_16x16x32_bf16 v[116:119], a[68:71], a[16:19], v[116:119]\n\t"
      "v_mfma_f32_16x16x32_bf16 v[120:123], a[72:75], a[16:19], v[120:123]\n\t"
      "s_waitcnt vmcnt(16) lgkmcnt(0)\n\t"
      "s_barrier\n\t"
      "v_mfma_f32_16x16x32_bf16 v[124:127], a[76:79], a[16:19], v[124:127]\n\t"
      "v_mfma_f32_16x16x32_bf16 v[128:131], a[80:83], a[16:19], v[128:131]\n\t"
      "s_add_u32 m0, s43, 0\n\t"
      "v_mfma_f32_16x16x32_bf16 v[132:135], a[84:87], a[16:19], v[132:135]\n\t"
      "buffer_load_dwordx4 v232, %[srd], s40 offen offset:0 lds\n\t"
      "v_mfma_f32_16x16x32_bf16 v[136:139], a[88:91], a[16:19], v[136:139]\n\t"
      "v_mfma_f32_16x16x32_bf16 v[164:167], a[88:91], a[20:23], v[164:167]\n\t"
      "v_mfma_f32_16x16x32_bf16 v[160:163], a[84:87], a[20:23], v[160:163]\n\t"
      "v_mfma_f32_16x16x32_bf16 v[156:159], a[80:83], a[20:23], v[156:159]\n\t"
      "v_mfma_f32_16x16x32_bf16 v[152:155], a[76:79], a[20:23], v[152:155]\n\t"
      "buffer_load_dwordx4 v233, %[srd], s40 offen offset:1024 lds\n\t"
      "v_mfma_f32_16x16x32_bf16 v[148:151], a[72:75], a[20:23], v[148:151]\n\t"
      "v_mfma_f32_16x16x32_bf16 v[144:147], a[68:71], a[20:23], v[144:147]\n\t"
      "v_mfma_f32_16x16x32_bf16 v[140:143], a[64:67], a[20:23], v[140:143]\n\t"
      "v_mfma_f32_16x16x32_bf16 v[168:171], a[64:67], a[24:27], v[168:171]\n\t"
      "v_mfma_f32_16x16x32_bf16 v[172:175], a[68:71], a[24:27], v[172:175]\n\t"
      "buffer_load_dwordx4 v234, %[srd], s40 offen offset:2048 lds\n\t"
      "v_mfma_f32_16x16x32_bf16 v[176:179], a[72:75], a[24:27], v[176:179]\n\t"
      "v_mfma_f32_16x16x32_bf16 v[180:183], a[76:79], a[24:27], v[180:183]\n\t"
      "v_mfma_f32_16x16x32_bf16 v[184:187], a[80:83], a[24:27], v[184:187]\n\t"
      "v_mfma_f32_16x16x32_bf16 v[188:191], a[84:87], a[24:27], v[188:191]\n\t"
      "v_mfma_f32_16x16x32_bf16 v[192:195], a[88:91], a[24:27], v[192:195]\n\t"
      "buffer_load_dwordx4 v235, %[srd], s40 offen offset:3072 lds\n\t"
      "v_mfma_f32_16x16x32_bf16 v[220:223], a[88:91], a[28:31], v[220:223]\n\t"
      "v_mfma_f32_16x16x32_bf16 v[216:219], a[84:87], a[28:31], v[216:219]\n\t"
      "v_mfma_f32_16x16x32_bf16 v[212:215], a[80:83], a[28:31], v[212:215]\n\t"
      "v_mfma_f32_16x16x32_bf16 v[208:211], a[76:79], a[28:31], v[208:211]\n\t"
      "s_add_u32 m0, s43, 4096\n\t"
      "v_mfma_f32_16x16x32_bf16 v[204:207], a[72:75], a[28:31], v[204:207]\n\t"
      "buffer_load_dwordx4 v236, %[srd], s40 offen offset:0 lds\n\t"
      "v_mfma_f32_16x16x32_bf16 v[200:203], a[68:71], a[28:31], v[200:203]\n\t"
      "v_mfma_f32_16x16x32_bf16 v[196:199], a[64:67], a[28:31], v[196:199]\n\t"
      "v_mfma_f32_16x16x32_bf16 v[0:3], a[92:95], a[32:35], v[0:3]\n\t"
      "v_mfma_f32_16x16x32_bf16 v[4:7], a[96:99], a[32:35], v[4:7]\n\t"
      "v_mfma_f32_16x16x32_bf16 v[8:11], a[100:103], a[32:35], v[8:11]\n\t"
      "buffer_load_dwordx4 v237, %[srd], s40 offen offset:1024 lds\n\t"
      "v_mfma_f32_16x16x32_bf16 v[12:15], a[104:107], a[32:35], v[12:15]\n\t"
      "v_mfma_f32_16x16x32_bf16 v[16:19], a[108:111], a[32:35], v[16:19]\n\t"
      "v_mfma_f32_16x16x32_bf16 v[20:23], a[112:115], a[32:35], v[20:23]\n\t"
      "v_mfma_f32_16x16x32_bf16 v[24:27], a[116:119], a[32:35], v[24:27]\n\t"
      "v_mfma_f32_16x16x32_bf16 v[52:55], a[116:119], a[36:39], v[52:55]\n\t"
      "buffer_load_dwordx4 v238, %[srd], s40 offen offset:2048 lds\n\t"
      "v_mfma_f32_16x16x32_bf16 v[48:51], a[112:115], a[36:39], v[48:51]\n\t"
      "v_mfma_f32_16x16x32_bf16 v[44:47], a[108:111], a[36:39], v[44:47]\n\t"
      "v_mfma_f32_16x16x32_bf16 v[40:43], a[104:107], a[36:39], v[40:43]\n\t"
      "v_mfma_f32_16x16x32_bf16 v[36:39], a[100:103], a[36:39], v[36:39]\n\t"
      "s_add_u32 m0, s43, 7168\n\t"
      "v_mfma_f32_16x16x32_bf16 v[32:35], a[96:99], a[36:39], v[32:35]\n\t"
      "buffer_load_dwordx4 v232, %[srd], s41 offen offset:0 lds\n\t"
      "v_mfma_f32_16x16x32_bf16 v[28:31], a[92:95], a[36:39], v[28:31]\n\t"
      "v_mfma_f32_16x16x32_bf16 v[56:59], a[92:95], a[40:43], v[56:59]\n\t"
      "v_mfma_f32_16x16x32_bf16 v[60:63], a[96:99], a[40:43], v[60:63]\n\t"
      "v_mfma_f32_16x16x32_bf16 v[64:67], a[100:103], a[40:43], v[64:67]\n\t"
      "v_mfma_f32_16x16x32_bf16 v[68:71], a[104:107], a[40:43], v[68:71]\n\t"
      "buffer_load_dwordx4 v233, %[srd], s41 offen offset:1024 lds\n\t"
      "v_mfma_f32_16x16x32_bf16 v[72:75], a[108:111], a[40:43], v[72:75]\n\t"
      "v_mfma_f32_16x16x32_bf16 v[76:79], a[112:115], a[40:43], v[76:79]\n\t"
      "v_mfma_f32_16x16x32_bf16 v[80:83], a[116:119], a[40:43], v[80:83]\n\t"
      "ds_read_b64_tr_b16 a[64:65], v226 offset:0\n\t"
      "v_mfma_f32_16x16x32_bf16 v[108:111], a[116:119], a[44:47], v[108:111]\n\t"
      "ds_read_b64_tr_b16 a[66:67], v226 offset:896\n\t"
      "v_mfma_f32_16x16x32_bf16 v[104:107], a[112:115], a[44:47], v[104:107]\n\t"
      "ds_read_b64_tr_b16 a[68:69], v227 offset:0\n\t"
      "buffer_load_dwordx4 v234, %[srd], s41 offen offset:2048 lds\n\t"
      "v_mfma_f32_16x16x32_bf16 v[100:103], a[108:111], a[44:47], v[100:103]\n\t"
      "ds_read_b64_tr_b16 a[70:71], v227 offset:896\n\t"
      "v_mfma_f32_16x16x32_bf16 v[96:99], a[104:107], a[44:47], v[96:99]\n\t"
      "ds_read_b64_tr_b16 a[72:73], v226 offset:32\n\t"
      "v_mfma_f32_16x16x32_bf16 v[92:95], a[100:103], a[44:47], v[92:95]\n\t"
      "ds_read_b64_tr_b16 a[74:75], v226 offset:928\n\t"
      "v_mfma_f32_16x16x32_bf16 v[88:91], a[96:99], a[44:47], v[88:91]\n\t"
      "ds_read_b64_tr_b16 a[76:77], v227 offset:32\n\t"
      "v_mfma_f32_16x16x32_bf16 v[84:87], a[92:95], a[44:47], v[84:87]\n\t"
      "ds_read_b64_tr_b16 a[78:79], v227 offset:928\n\t"
      "buffer_load_dwordx4 v235, %[srd], s41 offen offset:3072 lds\n\t"
      "v_mfma_f32_16x16x32_bf16 v[112:115], a[92:95], a[48:51], v[112:115]\n\t"
      "ds_read_b64_tr_b16 a[80:81], v226 offset:64\n\t"
      "v_mfma_f32_16x16x32_bf16 v[116:119], a[96:99], a[48:51], v[116:119]\n\t"
      "ds_read_b64_tr_b16 a[82:83], v226 offset:960\n\t"
      "v_mfma_f32_16x16x32_bf16 v[120:123], a[100:103], a[48:51], v[120:123]\n\t"
      "ds_read_b64_tr_b16 a[84:85], v227 offset:64\n\t"
      "v_mfma_f32_16x16x32_bf16 v[124:127], a[104:107], a[48:51], v[124:127]\n\t"
      "ds_read_b64_tr_b16 a[86:87], v227 offset:960\n\t"
      "s_add_u32 m0, s43, 11264\n\t"
      "v_mfma_f32_16x16x32_bf16 v[128:131], a[108:111], a[48:51], v[128:131]\n\t"
      "ds_read_b64_tr_b16 a[88:89], v228 offset:0\n\t"
      "buffer_load_dwordx4 v236, %[srd], s41 offen offset:0 lds\n\t"
      "v_mfma_f32_16x16x32_bf16 v[132:135], a[112:115], a[48:51], v[132:135]\n\t"
      "ds_read_b64_tr_b16 a[90:91], v228 offset:896\n\t"
      "v_mfma_f32_16x16x32_bf16 v[136:139], a[116:119], a[48:51], v[136:139]\n\t"
      "ds_read_b128 a[0:3], v224 offset:0\n\t"
      "v_mfma_f32_16x16x32_bf16 v[164:167], a[116:119], a[52:55], v[164:167]\n\t"
      "ds_read_b128 a[4:7], v224 offset:2048\n\t"
      "v_mfma_f32_16x16x32_bf16 v[160:163], a[112:115], a[52:55], v[160:163]\n\t"
      "ds_read_b128 a[8:11], v224 offset:4096\n\t"
      "v_mfma_f32_16x16x32_bf16 v[156:159], a[108:111], a[52:55], v[156:159]\n\t"
      "ds_read_b128 a[12:15], v224 offset:6144\n\t"
      "buffer_load_dwordx4 v237, %[srd], s41 offen offset:1024 lds\n\t"
      "v_mfma_f32_16x16x32_bf16 v[152:155], a[104:107], a[52:55], v[152:155]\n\t"
      "ds_read_b128 a[16:19], v224 offset:8192\n\t"
      "v_mfma_f32_16x16x32_bf16 v[148:151], a[100:103], a[52:55], v[148:151]\n\t"
      "ds_read_b128 a[20:23], v224 offset:10240\n\t"
      "v_mfma_f32_16x16x32_bf16 v[144:147], a[96:99], a[52:55], v[144:147]\n\t"
      "ds_read_b128 a[24:27], v224 offset:12288\n\t"
      "v_mfma_f32_16x16x32_bf16 v[140:143], a[92:95], a[52:55], v[140:143]\n\t"
      "ds_read_b128 a[28:31], v224 offset:14336\n\t"
      "v_mfma_f32_16x16x32_bf16 v[168:171], a[92:95], a[56:59], v[168:171]\n\t"
      "buffer_load_dwordx4 v238, %[srd], s41 offen offset:2048 lds\n\t"
      "v_mfma_f32_16x16x32_bf16 v[172:175], a[96:99], a[56:59], v[172:175]\n\t"
      "s_add_u32 s40, s40, 458752\n\t"
      "v_mfma_f32_16x16x32_bf16 v[176:179], a[100:103], a[56:59], v[176:179]\n\t"
      "s_add_u32 s41, s41, 458752\n\t"
      "v_mfma_f32_16x16x32_bf16 v[180:183], a[104:107], a[56:59], v[180:183]\n\t"
      "s_add_u32 s43, s43, s46\n\t"
      "v_mfma_f32_16x16x32_bf16 v[184:187], a[108:111], a[56:59], v[184:187]\n\t"
      "v_mfma_f32_16x16x32_bf16 v[188:191], a[112:115], a[56:59], v[188:191]\n\t"
      "v_mfma_f32_16x16x32_bf16 v[192:195], a[116:119], a[56:59], v[192:195]\n\t"
      "v_mfma_f32_16x16x32_bf16 v[220:223], a[116:119], a[60:63], v[220:223]\n\t"
      "v_mfma_f32_16x16x32_bf16 v[216:219], a[112:115], a[60:63], v[216:219]\n\t"
      "v_mfma_f32_16x16x32_bf16 v[212:215], a[108:111], a[60:63], v[212:215]\n\t"
      "v_mfma_f32_16x16x32_bf16 v[208:211], a[104:107], a[60:63], v[208:211]\n\t"
      "s_sub_u32 s47, 0, s47\n\t"
      "v_mfma_f32_16x16x32_bf16 v[204:207], a[100:103], a[60:63], v[204:207]\n\t"
      "v_mfma_f32_16x16x32_bf16 v[200:203], a[96:99], a[60:63], v[200:203]\n\t"
      "s_waitcnt lgkmcnt(0)\n\t"
      "v_mfma_f32_16x16x32_bf16 v[196:199], a[92:95], a[60:63], v[196:199]\n\t"
      "v_mfma_f32_16x16x32_bf16 v[0:3], a[64:67], a[0:3], v[0:3]\n\t"
      "ds_read_b64_tr_b16 a[92:93], v226 offset:7168\n\t"
      "v_mfma_f32_16x16x32_bf16 v[4:7], a[68:71], a[0:3], v[4:7]\n\t"
      "ds_read_b64_tr_b16 a[94:95], v226 offset:8064\n\t"
      "v_mfma_f32_16x16x32_bf16 v[8:11], a[72:75], a[0:3], v[8:11]\n\t"
      "ds_read_b64_tr_b16 a[96:97], v227 offset:7168\n\t"
      "v_mfma_f32_16x16x32_bf16 v[12:15], a[76:79], a[0:3], v[12:15]\n\t"
      "ds_read_b64_tr_b16 a[98:99], v227 offset:8064\n\t"
      "v_mfma_f32_16x16x32_bf16 v[16:19], a[80:83], a[0:3], v[16:19]\n\t"
      "ds_read_b64_tr_b16 a[100:101], v226 offset:7200\n\t"
      "v_mfma_f32_16x16x32_bf16 v[20:23], a[84:87], a[0:3], v[20:23]\n\t"
      "ds_read_b64_tr_b16 a[102:103], v226 offset:8096\n\t"
      "v_mfma_f32_16x16x32_bf16 v[24:27], a[88:91], a[0:3], v[24:27]\n\t"
      "ds_read_b64_tr_b16 a[104:105], v227 offset:7200\n\t"
      "v_mfma_f32_16x16x32_bf16 v[52:55], a[88:91], a[4:7], v[52:55]\n\t"
      "ds_read_b64_tr_b16 a[106:107], v227 offset:8096\n\t"
      "v_mfma_f32_16x16x32_bf16 v[48:51], a[84:87], a[4:7], v[48:51]\n\t"
      "ds_read_b64_tr_b16 a[108:109], v226 offset:7232\n\t"
      "v_mfma_f32_16x16x32_bf16 v[44:47], a[80:83], a[4:7], v[44:47]\n\t"
      "ds_read_b64_tr_b16 a[110:111], v226 offset:8128\n\t"
      "v_mfma_f32_16x16x32_bf16 v[40:43], a[76:79], a[4:7], v[40:43]\n\t"
      "ds_read_b64_tr_b16 a[112:113], v227 offset:7232\n\t"
      "v_mfma_f32_16x16x32_bf16 v[36:39], a[72:75], a[4:7], v[36:39]\n\t"
      "ds_read_b64_tr_b16 a[114:115], v227 offset:8128\n\t"
      "v_mfma_f32_16x16x32_bf16 v[32:35], a[68:71], a[4:7], v[32:35]\n\t"
      "ds_read_b64_tr_b16 a[116:117], v228 offset:7168\n\t"
      "v_mfma_f32_16x16x32_bf16 v[28:31], a[64:67], a[4:7], v[28:31]\n\t"
      "ds_read_b64_tr_b16 a[118:119], v228 offset:8064\n\t"
      "v_mfma_f32_16x16x32_bf16 v[56:59], a[64:67], a[8:11], v[56:59]\n\t"
      "ds_read_b128 a[32:35], v225 offset:0\n\t"
      "v_mfma_f32_16x16x32_bf16 v[60:63], a[68:71], a[8:11], v[60:63]\n\t"
      "ds_read_b128 a[36:39], v225 offset:2048\n\t"
      "v_mfma_f32_16x16x32_bf16 v[64:67], a[72:75], a[8:11], v[64:67]\n\t"
      "ds_read_b128 a[40:43], v225 offset:4096\n\t"
      "v_mfma_f32_16x16x32_bf16 v[68:71], a[76:79], a[8:11], v[68:71]\n\t"
      "ds_read_b128 a[44:47], v225 offset:6144\n\t"
      "v_mfma_f32_16x16x32_bf16 v[72:75], a[80:83], a[8:11], v[72:75]\n\t"
      "ds_read_b128 a[48:51], v225 offset:8192\n\t"
      "v_mfma_f32_16x16x32_bf16 v[76:79], a[84:87], a[8:11], v[76:79]\n\t"
      "ds_read_b128 a[52:55], v225 offset:10240\n\t"
      "v_mfma_f32_16x16x32_bf16 v[80:83], a[88:91], a[8:11], v[80:83]\n\t"
      "ds_read_b128 a[56:59], v225 offset:12288\n\t"
      "v_mfma_f32_16x16x32_bf16 v[108:111], a[88:91], a[12:15], v[108:111]\n\t"
      "ds_read_b128 a[60:63], v225 offset:14336\n\t"
      "v_mfma_f32_16x16x32_bf16 v[104:107], a[84:87], a[12:15], v[104:107]\n\t"
      "s_add_u32 s44, s44, 1\n\t"
      "v_mfma_f32_16x16x32_bf16 v[100:103], a[80:83], a[12:15], v[100:103]\n\t"
      "s_cmp_eq_u32 s44, 3\n\t"
      "s_cselect_b32 s46, s49, s48\n\t"
      "s_cselect_b32 s44, 0, s44\n\t"
      "v_mfma_f32_16x16x32_bf16 v[96:99], a[76:79], a[12:15], v[96:99]\n\t"
      "v_add_u32 v224, s47, v224\n\t"
      "v_mfma_f32_16x16x32_bf16 v[92:95], a[72:75], a[12:15], v[92:95]\n\t"
      "v_add_u32 v225, s47, v225\n\t"
      "v_mfma_f32_16x16x32_bf16 v[88:91], a[68:71], a[12:15], v[88:91]\n\t"
      "v_add_u32 v226, s46, v226\n\t"
      "v_mfma_f32_16x16x32_bf16 v[84:87], a[64:67], a[12:15], v[84:87]\n\t"
      "v_add_u32 v227, s46, v227\n\t"
      "v_mfma_f32_16x16x32_bf16 v[112:115], a[64:67], a[16:19], v[112:115]\n\t"
      "v_add_u32 v228, s46, v228\n\t"
      "v_mfma_f32_16x16x32_bf16 v[116:119], a[68:71], a[16:19], v[116:119]\n\t"
      "v_mfma_f32_16x16x32_bf16 v[120:123], a[72:75], a[16:19], v[120:123]\n\t"
      "s_waitcnt vmcnt(15) lgkmcnt(0)\n\t"
      "s_barrier\n\t"
      "v_mfma_f32_16x16x32_bf16 v[124:127], a[76:79], a[16:19], v[124:127]\n\t"
      "v_mfma_f32_16x16x32_bf16 v[128:131], a[80:83], a[16:19], v[128:131]\n\t"
      "s_add_u32 m0, s43, 0\n\t"
      "v_mfma_f32_16x16x32_bf16 v[132:135], a[84:87], a[16:19], v[132:135]\n\t"
      "buffer_load_dwordx4 v232, %[srd], s40 offen offset:0 lds\n\t"
      "v_mfma_f32_16x16x32_bf16 v[136:139], a[88:91], a[16:19], v[136:139]\n\t"
      "v_mfma_f32_16x16x32_bf16 v[164:167], a[88:91], a[20:23], v[164:167]\n\t"
      "v_mfma_f32_16x16x32_bf16 v[160:163], a[84:87], a[20:23], v[160:163]\n\t"
      "v_mfma_f32_16x16x32_bf16 v[156:159], a[80:83], a[20:23], v[156:159]\n\t"
      "v_mfma_f32_16x16x32_bf16 v[152:155], a[76:79], a[20:23], v[152:155]\n\t"
      "buffer_load_dwordx4 v233, %[srd], s40 offen offset:1024 lds\n\t"
      "v_mfma_f32_16x16x32_bf16 v[148:151], a[72:75], a[20:23], v[148:151]\n\t"
      "v_mfma_f32_16x16x32_bf16 v[144:147], a[68:71], a[20:23], v[144:147]\n\t"
      "v_mfma_f32_16x16x32_bf16 v[140:143], a[64:67], a[20:23], v[140:143]\n\t"
      "v_mfma_f32_16x16x32_bf16 v[168:171], a[64:67], a[24:27], v[168:171]\n\t"
      "v_mfma_f32_16x16x32_bf16 v[172:175], a[68:71], a[24:27], v[172:175]\n\t"
      "buffer_load_dwordx4 v234, %[srd], s40 offen offset:2048 lds\n\t"
      "v_mfma_f32_16x16x32_bf16 v[176:179], a[72:75], a[24:27], v[176:179]\n\t"
      "v_mfma_f32_16x16x32_bf16 v[180:183], a[76:79], a[24:27], v[180:183]\n\t"
      "v_mfma_f32_16x16x32_bf16 v[184:187], a[80:83], a[24:27], v[184:187]\n\t"
      "v_mfma_f32_16x16x32_bf16 v[188:191], a[84:87], a[24:27], v[188:191]\n\t"
      "v_mfma_f32_16x16x32_bf16 v[192:195], a[88:91], a[24:27], v[192:195]\n\t"
      "buffer_load_dwordx4 v235, %[srd], s40 offen offset:3072 lds\n\t"
      "v_mfma_f32_16x16x32_bf16 v[220:223], a[88:91], a[28:31], v[220:223]\n\t"
      "v_mfma_f32_16x16x32_bf16 v[216:219], a[84:87], a[28:31], v[216:219]\n\t"
      "v_mfma_f32_16x16x32_bf16 v[212:215], a[80:83], a[28:31], v[212:215]\n\t"
      "v_mfma_f32_16x16x32_bf16 v[208:211], a[76:79], a[28:31], v[208:211]\n\t"
      "s_add_u32 m0, s43, 4096\n\t"
      "v_mfma_f32_16x16x32_bf16 v[204:207], a[72:75], a[28:31], v[204:207]\n\t"
      "buffer_load_dwordx4 v236, %[srd], s40 offen offset:0 lds\n\t"
      "v_mfma_f32_16x16x32_bf16 v[200:203], a[68:71], a[28:31], v[200:203]\n\t"
      "v_mfma_f32_16x16x32_bf16 v[196:199], a[64:67], a[28:31], v[196:199]\n\t"
      "v_mfma_f32_16x16x32_bf16 v[0:3], a[92:95], a[32:35], v[0:3]\n\t"
      "v_mfma_f32_16x16x32_bf16 v[4:7], a[96:99], a[32:35], v[4:7]\n\t"
      "v_mfma_f32_16x16x32_bf16 v[8:11], a[100:103], a[32:35], v[8:11]\n\t"
      "buffer_load_dwordx4 v237, %[srd], s40 offen offset:1024 lds\n\t"
      "v_mfma_f32_16x16x32_bf16 v[12:15], a[104:107], a[32:35], v[12:15]\n\t"
      "v_mfma_f32_16x16x32_bf16 v[16:19], a[108:111], a[32:35], v[16:19]\n\t"
      "v_mfma_f32_16x16x32_bf16 v[20:23], a[112:115], a[32:35], v[20:23]\n\t"
      "v_mfma_f32_16x16x32_bf16 v[24:27], a[116:119], a[32:35], v[24:27]\n\t"
      "v_mfma_f32_16x16x32_bf16 v[52:55], a[116:119], a[36:39], v[52:55]\n\t"
      "buffer_load_dwordx4 v238, %[srd], s40 offen offset:2048 lds\n\t"
      "v_mfma_f32_16x16x32_bf16 v[48:51], a[112:115], a[36:39], v[48:51]\n\t"
      "v_mfma_f32_16x16x32_bf16 v[44:47], a[108:111], a[36:39], v[44:47]\n\t"
      "v_mfma_f32_16x16x32_bf16 v[40:43], a[104:107], a[36:39], v[40:43]\n\t"
      "v_mfma_f32_16x16x32_bf16 v[36:39], a[100:103], a[36:39], v[36:39]\n\t"
      "s_add_u32 m0, s43, 7168\n\t"
      "v_mfma_f32_16x16x32_bf16 v[32:35], a[96:99], a[36:39], v[32:35]\n\t"
      "buffer_load_dwordx4 v232, %[srd], s41 offen offset:0 lds\n\t"
      "v_mfma_f32_16x16x32_bf16 v[28:31], a[92:95], a[36:39], v[28:31]\n\t"
      "v_mfma_f32_16x16x32_bf16 v[56:59], a[92:95], a[40:43], v[56:59]\n\t"
      "v_mfma_f32_16x16x32_bf16 v[60:63], a[96:99], a[40:43], v[60:63]\n\t"
      "v_mfma_f32_16x16x32_bf16 v[64:67], a[100:103], a[40:43], v[64:67]\n\t"
      "v_mfma_f32_16x16x32_bf16 v[68:71], a[104:107], a[40:43], v[68:71]\n\t"
      "buffer_load_dwordx4 v233, %[srd], s41 offen offset:1024 lds\n\t"
      "v_mfma_f32_16x16x32_bf16 v[72:75], a[108:111], a[40:43], v[72:75]\n\t"
      "v_mfma_f32_16x16x32_bf16 v[76:79], a[112:115], a[40:43], v[76:79]\n\t"
      "v_mfma_f32_16x16x32_bf16 v[80:83], a[116:119], a[40:43], v[80:83]\n\t"
      "ds_read_b64_tr_b16 a[64:65], v226 offset:0\n\t"
      "v_mfma_f32_16x16x32_bf16 v[108:111], a[116:119], a[44:47], v[108:111]\n\t"
      "ds_read_b64_tr_b16 a[66:67], v226 offset:896\n\t"
      "v_mfma_f32_16x16x32_bf16 v[104:107], a[112:115], a[44:47], v[104:107]\n\t"
      "ds_read_b64_tr_b16 a[68:69], v227 offset:0\n\t"
      "buffer_load_dwordx4 v234, %[srd], s41 offen offset:2048 lds\n\t"
      "v_mfma_f32_16x16x32_bf16 v[100:103], a[108:111], a[44:47], v[100:103]\n\t"
      "ds_read_b64_tr_b16 a[70:71], v227 offset:896\n\t"
      "v_mfma_f32_16x16x32_bf16 v[96:99], a[104:107], a[44:47], v[96:99]\n\t"
      "ds_read_b64_tr_b16 a[72:73], v226 offset:32\n\t"
      "v_mfma_f32_16x16x32_bf16 v[92:95], a[100:103], a[44:47], v[92:95]\n\t"
      "ds_read_b64_tr_b16 a[74:75], v226 offset:928\n\t"
      "v_mfma_f32_16x16x32_bf16 v[88:91], a[96:99], a[44:47], v[88:91]\n\t"
      "ds_read_b64_tr_b16 a[76:77], v227 offset:32\n\t"
      "v_mfma_f32_16x16x32_bf16 v[84:87], a[92:95], a[44:47], v[84:87]\n\t"
      "ds_read_b64_tr_b16 a[78:79], v227 offset:928\n\t"
      "buffer_load_dwordx4 v235, %[srd], s41 offen offset:3072 lds\n\t"
      "v_mfma_f32_16x16x32_bf16 v[112:115], a[92:95], a[48:51], v[112:115]\n\t"
      "ds_read_b64_tr_b16 a[80:81], v226 offset:64\n\t"
      "v_mfma_f32_16x16x32_bf16 v[116:119], a[96:99], a[48:51], v[116:119]\n\t"
      "ds_read_b64_tr_b16 a[82:83], v226 offset:960\n\t"
      "v_mfma_f32_16x16x32_bf16 v[120:123], a[100:103], a[48:51], v[120:123]\n\t"
      "ds_read_b64_tr_b16 a[84:85], v227 offset:64\n\t"
      "v_mfma_f32_16x16x32_bf16 v[124:127], a[104:107], a[48:51], v[124:127]\n\t"
      "ds_read_b64_tr_b16 a[86:87], v227 offset:960\n\t"
      "s_add_u32 m0, s43, 11264\n\t"
      "v_mfma_f32_16x16x32_bf16 v[128:131], a[108:111], a[48:51], v[128:131]\n\t"
      "ds_read_b64_tr_b16 a[88:89], v228 offset:0\n\t"
      "buffer_load_dwordx4 v236, %[srd], s41 offen offset:0 lds\n\t"
      "v_mfma_f32_16x16x32_bf16 v[132:135], a[112:115], a[48:51], v[132:135]\n\t"
      "ds_read_b64_tr_b16 a[90:91], v228 offset:896\n\t"
      "v_mfma_f32_16x16x32_bf16 v[136:139], a[116:119], a[48:51], v[136:139]\n\t"
      "ds_read_b128 a[0:3], v224 offset:0\n\t"
      "v_mfma_f32_16x16x32_bf16 v[164:167], a[116:119], a[52:55], v[164:167]\n\t"
      "ds_read_b128 a[4:7], v224 offset:2048\n\t"
      "v_mfma_f32_16x16x32_bf16 v[160:163], a[112:115], a[52:55], v[160:163]\n\t"
      "ds_read_b128 a[8:11], v224 offset:4096\n\t"
      "v_mfma_f32_16x16x32_bf16 v[156:159], a[108:111], a[52:55], v[156:159]\n\t"
      "ds_read_b128 a[12:15], v224 offset:6144\n\t"
      "buffer_load_dwordx4 v237, %[srd], s41 offen offset:1024 lds\n\t"
      "v_mfma_f32_16x16x32_bf16 v[152:155], a[104:107], a[52:55], v[152:155]\n\t"
      "ds_read_b128 a[16:19], v224 offset:8192\n\t"
      "v_mfma_f32_16x16x32_bf16 v[148:151], a[100:103], a[52:55], v[148:151]\n\t"
      "ds_read_b128 a[20:23], v224 offset:10240\n\t"
      "v_mfma_f32_16x16x32_bf16 v[144:147], a[96:99], a[52:55], v[144:147]\n\t"
      "ds_read_b128 a[24:27], v224 offset:12288\n\t"
      "v_mfma_f32_16x16x32_bf16 v[140:143], a[92:95], a[52:55], v[140:143]\n\t"
      "ds_read_b128 a[28:31], v224 offset:14336\n\t"
      "v_mfma_f32_16x16x32_bf16 v[168:171], a[92:95], a[56:59], v[168:171]\n\t"
      "buffer_load_dwordx4 v238, %[srd], s41 offen offset:2048 lds\n\t"
      "v_mfma_f32_16x16x32_bf16 v[172:175], a[96:99], a[56:59], v[172:175]\n\t"
      "s_add_u32 s40, s40, 458752\n\t"
      "v_mfma_f32_16x16x32_bf16 v[176:179], a[100:103], a[56:59], v[176:179]\n\t"
      "s_add_u32 s41, s41, 458752\n\t"
      "v_mfma_f32_16x16x32_bf16 v[180:183], a[104:107], a[56:59], v[180:183]\n\t"
      "s_add_u32 s43, s43, s46\n\t"
      "v_mfma_f32_16x16x32_bf16 v[184:187], a[108:111], a[56:59], v[184:187]\n\t"
      "v_mfma_f32_16x16x32_bf16 v[188:191], a[112:115], a[56:59], v[188:191]\n\t"
      "v_mfma_f32_16x16x32_bf16 v[192:195], a[116:119], a[56:59], v[192:195]\n\t"
      "v_mfma_f32_16x16x32_bf16 v[220:223], a[116:119], a[60:63], v[220:223]\n\t"
      "v_mfma_f32_16x16x32_bf16 v[216:219], a[112:115], a[60:63], v[216:219]\n\t"
      "v_mfma_f32_16x16x32_bf16 v[212:215], a[108:111], a[60:63], v[212:215]\n\t"
      "v_mfma_f32_16x16x32_bf16 v[208:211], a[104:107], a[60:63], v[208:211]\n\t"
      "s_sub_u32 s47, 0, s47\n\t"
      "v_mfma_f32_16x16x32_bf16 v[204:207], a[100:103], a[60:63], v[204:207]\n\t"
      "v_mfma_f32_16x16x32_bf16 v[200:203], a[96:99], a[60:63], v[200:203]\n\t"
      "s_waitcnt lgkmcnt(0)\n\t"
      "v_mfma_f32_16x16x32_bf16 v[196:199], a[92:95], a[60:63], v[196:199]\n\t"
      "v_mfma_f32_16x16x32_bf16 v[0:3], a[64:67], a[0:3], v[0:3]\n\t"
      "ds_read_b64_tr_b16 a[92:93], v226 offset:7168\n\t"
      "v_mfma_f32_16x16x32_bf16 v[4:7], a[68:71], a[0:3], v[4:7]\n\t"
      "ds_read_b64_tr_b16 a[94:95], v226 offset:8064\n\t"
      "v_mfma_f32_16x16x32_bf16 v[8:11], a[72:75], a[0:3], v[8:11]\n\t"
      "ds_read_b64_tr_b16 a[96:97], v227 offset:7168\n\t"
      "v_mfma_f32_16x16x32_bf16 v[12:15], a[76:79], a[0:3], v[12:15]\n\t"
      "ds_read_b64_tr_b16 a[98:99], v227 offset:8064\n\t"
      "v_mfma_f32_16x16x32_bf16 v[16:19], a[80:83], a[0:3], v[16:19]\n\t"
      "ds_read_b64_tr_b16 a[100:101], v226 offset:7200\n\t"
      "v_mfma_f32_16x16x32_bf16 v[20:23], a[84:87], a[0:3], v[20:23]\n\t"
      "ds_read_b64_tr_b16 a[102:103], v226 offset:8096\n\t"
      "v_mfma_f32_16x16x32_bf16 v[24:27], a[88:91], a[0:3], v[24:27]\n\t"
      "ds_read_b64_tr_b16 a[104:105], v227 offset:7200\n\t"
      "v_mfma_f32_16x16x32_bf16 v[52:55], a[88:91], a[4:7], v[52:55]\n\t"
      "ds_read_b64_tr_b16 a[106:107], v227 offset:8096\n\t"
      "v_mfma_f32_16x16x32_bf16 v[48:51], a[84:87], a[4:7], v[48:51]\n\t"
      "ds_read_b64_tr_b16 a[108:109], v226 offset:7232\n\t"
      "v_mfma_f32_16x16x32_bf16 v[44:47], a[80:83], a[4:7], v[44:47]\n\t"
      "ds_read_b64_tr_b16 a[110:111], v226 offset:8128\n\t"
      "v_mfma_f32_16x16x32_bf16 v[40:43], a[76:79], a[4:7], v[40:43]\n\t"
      "ds_read_b64_tr_b16 a[112:113], v227 offset:7232\n\t"
      "v_mfma_f32_16x16x32_bf16 v[36:39], a[72:75], a[4:7], v[36:39]\n\t"
      "ds_read_b64_tr_b16 a[114:115], v227 offset:8128\n\t"
      "v_mfma_f32_16x16x32_bf16 v[32:35], a[68:71], a[4:7], v[32:35]\n\t"
      "ds_read_b64_tr_b16 a[116:117], v228 offset:7168\n\t"
      "v_mfma_f32_16x16x32_bf16 v[28:31], a[64:67], a[4:7], v[28:31]\n\t"
      "ds_read_b64_tr_b16 a[118:119], v228 offset:8064\n\t"
      "v_mfma_f32_16x16x32_bf16 v[56:59], a[64:67], a[8:11], v[56:59]\n\t"
      "ds_read_b128 a[32:35], v225 offset:0\n\t"
      "v_mfma_f32_16x16x32_bf16 v[60:63], a[68:71], a[8:11], v[60:63]\n\t"
      "ds_read_b128 a[36:39], v225 offset:2048\n\t"
      "v_mfma_f32_16x16x32_bf16 v[64:67], a[72:75], a[8:11], v[64:67]\n\t"
      "ds_read_b128 a[40:43], v225 offset:4096\n\t"
      "v_mfma_f32_16x16x32_bf16 v[68:71], a[76:79], a[8:11], v[68:71]\n\t"
      "ds_read_b128 a[44:47], v225 offset:6144\n\t"
      "v_mfma_f32_16x16x32_bf16 v[72:75], a[80:83], a[8:11], v[72:75]\n\t"
      "ds_read_b128 a[48:51], v225 offset:8192\n\t"
      "v_mfma_f32_16x16x32_bf16 v[76:79], a[84:87], a[8:11], v[76:79]\n\t"
      "ds_read_b128 a[52:55], v225 offset:10240\n\t"
      "v_mfma_f32_16x16x32_bf16 v[80:83], a[88:91], a[8:11], v[80:83]\n\t"
      "ds_read_b128 a[56:59], v225 offset:12288\n\t"
      "v_mfma_f32_16x16x32_bf16 v[108:111], a[88:91], a[12:15], v[108:111]\n\t"
      "ds_read_b128 a[60:63], v225 offset:14336\n\t"
      "v_mfma_f32_16x16x32_bf16 v[104:107], a[84:87], a[12:15], v[104:107]\n\t"
      "s_add_u32 s44, s44, 1\n\t"
      "v_mfma_f32_16x16x32_bf16 v[100:103], a[80:83], a[12:15], v[100:103]\n\t"
      "s_cmp_eq_u32 s44, 3\n\t"
      "s_cselect_b32 s46, s49, s48\n\t"
      "s_cselect_b32 s44, 0, s44\n\t"
      "v_mfma_f32_16x16x32_bf16 v[96:99], a[76:79], a[12:15], v[96:99]\n\t"
      "v_add_u32 v224, s47, v224\n\t"
      "v_mfma_f32_16x16x32_bf16 v[92:95], a[72:75], a[12:15], v[92:95]\n\t"
      "v_add_u32 v225, s47, v225\n\t"
      "v_mfma_f32_16x16x32_bf16 v[88:91], a[68:71], a[12:15], v[88:91]\n\t"
      "v_add_u32 v226, s46, v226\n\t"
      "v_mfma_f32_16x16x32_bf16 v[84:87], a[64:67], a[12:15], v[84:87]\n\t"
      "v_add_u32 v227, s46, v227\n\t"
      "v_mfma_f32_16x16x32_bf16 v[112:115], a[64:67], a[16:19], v[112:115]\n\t"
      "v_add_u32 v228, s46, v228\n\t"
      "v_mfma_f32_16x16x32_bf16 v[116:119], a[68:71], a[16:19], v[116:119]\n\t"
      "v_mfma_f32_16x16x32_bf16 v[120:123], a[72:75], a[16:19], v[120:123]\n\t"
      "s_waitcnt vmcnt(14) lgkmcnt(0)\n\t"
      "s_barrier\n\t"
      "v_mfma_f32_16x16x32_bf16 v[124:127], a[76:79], a[16:19], v[124:127]\n\t"
      "v_mfma_f32_16x16x32_bf16 v[128:131], a[80:83], a[16:19], v[128:131]\n\t"
      "v_mfma_f32_16x16x32_bf16 v[132:135], a[84:87], a[16:19], v[132:135]\n\t"
      "v_mfma_f32_16x16x32_bf16 v[136:139], a[88:91], a[16:19], v[136:139]\n\t"
      "v_mfma_f32_16x16x32_bf16 v[164:167], a[88:91], a[20:23], v[164:167]\n\t"
      "v_mfma_f32_16x16x32_bf16 v[160:163], a[84:87], a[20:23], v[160:163]\n\t"
      "v_mfma_f32_16x16x32_bf16 v[156:159], a[80:83], a[20:23], v[156:159]\n\t"
      "v_mfma_f32_16x16x32_bf16 v[152:155], a[76:79], a[20:23], v[152:155]\n\t"
      "v_mfma_f32_16x16x32_bf16 v[148:151], a[72:75], a[20:23], v[148:151]\n\t"
      "v_mfma_f32_16x16x32_bf16 v[144:147], a[68:71], a[20:23], v[144:147]\n\t"
      "v_mfma_f32_16x16x32_bf16 v[140:143], a[64:67], a[20:23], v[140:143]\n\t"
      "v_mfma_f32_16x16x32_bf16 v[168:171], a[64:67], a[24:27], v[168:171]\n\t"
      "v_mfma_f32_16x16x32_bf16 v[172:175], a[68:71], a[24:27], v[172:175]\n\t"
      "v_mfma_f32_16x16x32_bf16 v[176:179], a[72:75], a[24:27], v[176:179]\n\t"
      "v_mfma_f32_16x16x32_bf16 v[180:183], a[76:79], a[24:27], v[180:183]\n\t"
      "v_mfma_f32_16x16x32_bf16 v[184:187], a[80:83], a[24:27], v[184:187]\n\t"
      "v_mfma_f32_16x16x32_bf16 v[188:191], a[84:87], a[24:27], v[188:191]\n\t"
      "v_mfma_f32_16x16x32_bf16 v[192:195], a[88:91], a[24:27], v[192:195]\n\t"
      "v_mfma_f32_16x16x32_bf16 v[220:223], a[88:91], a[28:31], v[220:223]\n\t"
      "v_mfma_f32_16x16x32_bf16 v[216:219], a[84:87], a[28:31], v[216:219]\n\t"
      "v_mfma_f32_16x16x32_bf16 v[212:215], a[80:83], a[28:31], v[212:215]\n\t"
      "v_mfma_f32_16x16x32_bf16 v[208:211], a[76:79], a[28:31], v[208:211]\n\t"
      "v_mfma_f32_16x16x32_bf16 v[204:207], a[72:75], a[28:31], v[204:207]\n\t"
      "v_mfma_f32_16x16x32_bf16 v[200:203], a[68:71], a[28:31], v[200:203]\n\t"
      "v_mfma_f32_16x16x32_bf16 v[196:199], a[64:67], a[28:31], v[196:199]\n\t"
      "v_mfma_f32_16x16x32_bf16 v[0:3], a[92:95], a[32:35], v[0:3]\n\t"
      "v_mfma_f32_16x16x32_bf16 v[4:7], a[96:99], a[32:35], v[4:7]\n\t"
      "v_mfma_f32_16x16x32_bf16 v[8:11], a[100:103], a[32:35], v[8:11]\n\t"
      "v_mfma_f32_16x16x32_bf16 v[12:15], a[104:107], a[32:35], v[12:15]\n\t"
      "v_mfma_f32_16x16x32_bf16 v[16:19], a[108:111], a[32:35], v[16:19]\n\t"
      "v_mfma_f32_16x16x32_bf16 v[20:23], a[112:115], a[32:35], v[20:23]\n\t"
      "v_mfma_f32_16x16x32_bf16 v[24:27], a[116:119], a[32:35], v[24:27]\n\t"
      "v_mfma_f32_16x16x32_bf16 v[52:55], a[116:119], a[36:39], v[52:55]\n\t"
      "v_mfma_f32_16x16x32_bf16 v[48:51], a[112:115], a[36:39], v[48:51]\n\t"
      "v_mfma_f32_16x16x32_bf16 v[44:47], a[108:111], a[36:39], v[44:47]\n\t"
      "v_mfma_f32_16x16x32_bf16 v[40:43], a[104:107], a[36:39], v[40:43]\n\t"
      "v_mfma_f32_16x16x32_bf16 v[36:39], a[100:103], a[36:39], v[36:39]\n\t"
      "v_mfma_f32_16x16x32_bf16 v[32:35], a[96:99], a[36:39], v[32:35]\n\t"
      "v_mfma_f32_16x16x32_bf16 v[28:31], a[92:95], a[36:39], v[28:31]\n\t"
      "v_mfma_f32_16x16x32_bf16 v[56:59], a[92:95], a[40:43], v[56:59]\n\t"
      "v_mfma_f32_16x16x32_bf16 v[60:63], a[96:99], a[40:43], v[60:63]\n\t"
      "v_mfma_f32_16x16x32_bf16 v[64:67], a[100:103], a[40:43], v[64:67]\n\t"
      "v_mfma_f32_16x16x32_bf16 v[68:71], a[104:107], a[40:43], v[68:71]\n\t"
      "v_mfma_f32_16x16x32_bf16 v[72:75], a[108:111], a[40:43], v[72:75]\n\t"
      "v_mfma_f32_16x16x32_bf16 v[76:79], a[112:115], a[40:43], v[76:79]\n\t"
      "v_mfma_f32_16x16x32_bf16 v[80:83], a[116:119], a[40:43], v[80:83]\n\t"
      "ds_read_b64_tr_b16 a[64:65], v226 offset:0\n\t"
      "v_mfma_f32_16x16x32_bf16 v[108:111], a[116:119], a[44:47], v[108:111]\n\t"
      "ds_read_b64_tr_b16 a[66:67], v226 offset:896\n\t"
      "v_mfma_f32_16x16x32_bf16 v[104:107], a[112:115], a[44:47], v[104:107]\n\t"
      "ds_read_b64_tr_b16 a[68:69], v227 offset:0\n\t"
      "v_mfma_f32_16x16x32_bf16 v[100:103], a[108:111], a[44:47], v[100:103]\n\t"
      "ds_read_b64_tr_b16 a[70:71], v227 offset:896\n\t"
      "v_mfma_f32_16x16x32_bf16 v[96:99], a[104:107], a[44:47], v[96:99]\n\t"
      "ds_read_b64_tr_b16 a[72:73], v226 offset:32\n\t"
      "v_mfma_f32_16x16x32_bf16 v[92:95], a[100:103], a[44:47], v[92:95]\n\t"
      "ds_read_b64_tr_b16 a[74:75], v226 offset:928\n\t"
      "v_mfma_f32_16x16x32_bf16 v[88:91], a[96:99], a[44:47], v[88:91]\n\t"
      "ds_read_b64_tr_b16 a[76:77], v227 offset:32\n\t"
      "v_mfma_f32_16x16x32_bf16 v[84:87], a[92:95], a[44:47], v[84:87]\n\t"
      "ds_read_b64_tr_b16 a[78:79], v227 offset:928\n\t"
      "v_mfma_f32_16x16x32_bf16 v[112:115], a[92:95], a[48:51], v[112:115]\n\t"
      "ds_read_b64_tr_b16 a[80:81], v226 offset:64\n\t"
      "v_mfma_f32_16x16x32_bf16 v[116:119], a[96:99], a[48:51], v[116:119]\n\t"
      "ds_read_b64_tr_b16 a[82:83], v226 offset:960\n\t"
      "v_mfma_f32_16x16x32_bf16 v[120:123], a[100:103], a[48:51], v[120:123]\n\t"
      "ds_read_b64_tr_b16 a[84:85], v227 offset:64\n\t"
      "v_mfma_f32_16x16x32_bf16 v[124:127], a[104:107], a[48:51], v[124:127]\n\t"
      "ds_read_b64_tr_b16 a[86:87], v227 offset:960\n\t"
      "v_mfma_f32_16x16x32_bf16 v[128:131], a[108:111], a[48:51], v[128:131]\n\t"
      "ds_read_b64_tr_b16 a[88:89], v228 offset:0\n\t"
      "v_mfma_f32_16x16x32_bf16 v[132:135], a[112:115], a[48:51], v[132:135]\n\t"
      "ds_read_b64_tr_b16 a[90:91], v228 offset:896\n\t"
      "v_mfma_f32_16x16x32_bf16 v[136:139], a[116:119], a[48:51], v[136:139]\n\t"
      "ds_read_b128 a[0:3], v224 offset:0\n\t"
      "v_mfma_f32_16x16x32_bf16 v[164:167], a[116:119], a[52:55], v[164:167]\n\t"
      "ds_read_b128 a[4:7], v224 offset:2048\n\t"
      "v_mfma_f32_16x16x32_bf16 v[160:163], a[112:115], a[52:55], v[160:163]\n\t"
      "ds_read_b128 a[8:11], v224 offset:4096\n\t"
      "v_mfma_f32_16x16x32_bf16 v[156:159], a[108:111], a[52:55], v[156:159]\n\t"
      "ds_read_b128 a[12:15], v224 offset:6144\n\t"
      "v_mfma_f32_16x16x32_bf16 v[152:155], a[104:107], a[52:55], v[152:155]\n\t"
      "ds_read_b128 a[16:19], v224 offset:8192\n\t"
      "v_mfma_f32_16x16x32_bf16 v[148:151], a[100:103], a[52:55], v[148:151]\n\t"
      "ds_read_b128 a[20:23], v224 offset:10240\n\t"
      "v_mfma_f32_16x16x32_bf16 v[144:147], a[96:99], a[52:55], v[144:147]\n\t"
      "ds_read_b128 a[24:27], v224 offset:12288\n\t"
      "v_mfma_f32_16x16x32_bf16 v[140:143], a[92:95], a[52:55], v[140:143]\n\t"
      "ds_read_b128 a[28:31], v224 offset:14336\n\t"
      "v_mfma_f32_16x16x32_bf16 v[168:171], a[92:95], a[56:59], v[168:171]\n\t"
      "v_mfma_f32_16x16x32_bf16 v[172:175], a[96:99], a[56:59], v[172:175]\n\t"
      "v_mfma_f32_16x16x32_bf16 v[176:179], a[100:103], a[56:59], v[176:179]\n\t"
      "v_mfma_f32_16x16x32_bf16 v[180:183], a[104:107], a[56:59], v[180:183]\n\t"
      "v_mfma_f32_16x16x32_bf16 v[184:187], a[108:111], a[56:59], v[184:187]\n\t"
      "v_mfma_f32_16x16x32_bf16 v[188:191], a[112:115], a[56:59], v[188:191]\n\t"
      "v_mfma_f32_16x16x32_bf16 v[192:195], a[116:119], a[56:59], v[192:195]\n\t"
      "v_mfma_f32_16x16x32_bf16 v[220:223], a[116:119], a[60:63], v[220:223]\n\t"
      "v_mfma_f32_16x16x32_bf16 v[216:219], a[112:115], a[60:63], v[216:219]\n\t"
      "v_mfma_f32_16x16x32_bf16 v[212:215], a[108:111], a[60:63], v[212:215]\n\t"
      "v_mfma_f32_16x16x32_bf16 v[208:211], a[104:107], a[60:63], v[208:211]\n\t"
      "s_sub_u32 s47, 0, s47\n\t"
      "v_mfma_f32_16x16x32_bf16 v[204:207], a[100:103], a[60:63], v[204:207]\n\t"
      "v_mfma_f32_16x16x32_bf16 v[200:203], a[96:99], a[60:63], v[200:203]\n\t"
      "s_waitcnt lgkmcnt(0)\n\t"
      "v_mfma_f32_16x16x32_bf16 v[196:199], a[92:95], a[60:63], v[196:199]\n\t"
      "v_mfma_f32_16x16x32_bf16 v[0:3], a[64:67], a[0:3], v[0:3]\n\t"
      "ds_read_b64_tr_b16 a[92:93], v226 offset:7168\n\t"
      "v_mfma_f32_16x16x32_bf16 v[4:7], a[68:71], a[0:3], v[4:7]\n\t"
      "ds_read_b64_tr_b16 a[94:95], v226 offset:8064\n\t"
      "v_mfma_f32_16x16x32_bf16 v[8:11], a[72:75], a[0:3], v[8:11]\n\t"
      "ds_read_b64_tr_b16 a[96:97], v227 offset:7168\n\t"
      "v_mfma_f32_16x16x32_bf16 v[12:15], a[76:79], a[0:3], v[12:15]\n\t"
      "ds_read_b64_tr_b16 a[98:99], v227 offset:8064\n\t"
      "v_mfma_f32_16x16x32_bf16 v[16:19], a[80:83], a[0:3], v[16:19]\n\t"
      "ds_read_b64_tr_b16 a[100:101], v226 offset:7200\n\t"
      "v_mfma_f32_16x16x32_bf16 v[20:23], a[84:87], a[0:3], v[20:23]\n\t"
      "ds_read_b64_tr_b16 a[102:103], v226 offset:8096\n\t"
      "v_mfma_f32_16x16x32_bf16 v[24:27], a[88:91], a[0:3], v[24:27]\n\t"
      "ds_read_b64_tr_b16 a[104:105], v227 offset:7200\n\t"
      "v_mfma_f32_16x16x32_bf16 v[52:55], a[88:91], a[4:7], v[52:55]\n\t"
      "ds_read_b64_tr_b16 a[106:107], v227 offset:8096\n\t"
      "v_mfma_f32_16x16x32_bf16 v[48:51], a[84:87], a[4:7], v[48:51]\n\t"
      "ds_read_b64_tr_b16 a[108:109], v226 offset:7232\n\t"
      "v_mfma_f32_16x16x32_bf16 v[44:47], a[80:83], a[4:7], v[44:47]\n\t"
      "ds_read_b64_tr_b16 a[110:111], v226 offset:8128\n\t"
      "v_mfma_f32_16x16x32_bf16 v[40:43], a[76:79], a[4:7], v[40:43]\n\t"
      "ds_read_b64_tr_b16 a[112:113], v227 offset:7232\n\t"
      "v_mfma_f32_16x16x32_bf16 v[36:39], a[72:75], a[4:7], v[36:39]\n\t"
      "ds_read_b64_tr_b16 a[114:115], v227 offset:8128\n\t"
      "v_mfma_f32_16x16x32_bf16 v[32:35], a[68:71], a[4:7], v[32:35]\n\t"
      "ds_read_b64_tr_b16 a[116:117], v228 offset:7168\n\t"
      "v_mfma_f32_16x16x32_bf16 v[28:31], a[64:67], a[4:7], v[28:31]\n\t"
      "ds_read_b64_tr_b16 a[118:119], v228 offset:8064\n\t"
      "v_mfma_f32_16x16x32_bf16 v[56:59], a[64:67], a[8:11], v[56:59]\n\t"
      "ds_read_b128 a[32:35], v225 offset:0\n\t"
      "v_mfma_f32_16x16x32_bf16 v[60:63], a[68:71], a[8:11], v[60:63]\n\t"
      "ds_read_b128 a[36:39], v225 offset:2048\n\t"
      "v_mfma_f32_16x16x32_bf16 v[64:67], a[72:75], a[8:11], v[64:67]\n\t"
      "ds_read_b128 a[40:43], v225 offset:4096\n\t"
      "v_mfma_f32_16x16x32_bf16 v[68:71], a[76:79], a[8:11], v[68:71]\n\t"
      "ds_read_b128 a[44:47], v225 offset:6144\n\t"
      "v_mfma_f32_16x16x32_bf16 v[72:75], a[80:83], a[8:11], v[72:75]\n\t"
      "ds_read_b128 a[48:51], v225 offset:8192\n\t"
      "v_mfma_f32_16x16x32_bf16 v[76:79], a[84:87], a[8:11], v[76:79]\n\t"
      "ds_read_b128 a[52:55], v225 offset:10240\n\t"
      "v_mfma_f32_16x16x32_bf16 v[80:83], a[88:91], a[8:11], v[80:83]\n\t"
      "ds_read_b128 a[56:59], v225 offset:12288\n\t"
      "v_mfma_f32_16x16x32_bf16 v[108:111], a[88:91], a[12:15], v[108:111]\n\t"
      "ds_read_b128 a[60:63], v225 offset:14336\n\t"
      "v_mfma_f32_16x16x32_bf16 v[104:107], a[84:87], a[12:15], v[104:107]\n\t"
      "s_add_u32 s44, s44, 1\n\t"
      "v_mfma_f32_16x16x32_bf16 v[100:103], a[80:83], a[12:15], v[100:103]\n\t"
      "s_cmp_eq_u32 s44, 3\n\t"
      "s_cselect_b32 s46, s49, s48\n\t"
      "s_cselect_b32 s44, 0, s44\n\t"
      "v_mfma_f32_16x16x32_bf16 v[96:99], a[76:79], a[12:15], v[96:99]\n\t"
      "v_add_u32 v224, s47, v224\n\t"
      "v_mfma_f32_16x16x32_bf16 v[92:95], a[72:75], a[12:15], v[92:95]\n\t"
      "v_add_u32 v225, s47, v225\n\t"
      "v_mfma_f32_16x16x32_bf16 v[88:91], a[68:71], a[12:15], v[88:91]\n\t"
      "v_add_u32 v226, s46, v226\n\t"
      "v_mfma_f32_16x16x32_bf16 v[84:87], a[64:67], a[12:15], v[84:87]\n\t"
      "v_add_u32 v227, s46, v227\n\t"
      "v_mfma_f32_16x16x32_bf16 v[112:115], a[64:67], a[16:19], v[112:115]\n\t"
      "v_add_u32 v228, s46, v228\n\t"
      "v_mfma_f32_16x16x32_bf16 v[116:119], a[68:71], a[16:19], v[116:119]\n\t"
      "v_mfma_f32_16x16x32_bf16 v[120:123], a[72:75], a[16:19], v[120:123]\n\t"
      "s_waitcnt vmcnt(0) lgkmcnt(0)\n\t"
      "s_barrier\n\t"
      "v_mfma_f32_16x16x32_bf16 v[124:127], a[76:79], a[16:19], v[124:127]\n\t"
      "v_mfma_f32_16x16x32_bf16 v[128:131], a[80:83], a[16:19], v[128:131]\n\t"
      "v_mfma_f32_16x16x32_bf16 v[132:135], a[84:87], a[16:19], v[132:135]\n\t"
      "v_mfma_f32_16x16x32_bf16 v[136:139], a[88:91], a[16:19], v[136:139]\n\t"
      "v_mfma_f32_16x16x32_bf16 v[164:167], a[88:91], a[20:23], v[164:167]\n\t"
      "v_mfma_f32_16x16x32_bf16 v[160:163], a[84:87], a[20:23], v[160:163]\n\t"
      "v_mfma_f32_16x16x32_bf16 v[156:159], a[80:83], a[20:23], v[156:159]\n\t"
      "v_mfma_f32_16x16x32_bf16 v[152:155], a[76:79], a[20:23], v[152:155]\n\t"
      "v_mfma_f32_16x16x32_bf16 v[148:151], a[72:75], a[20:23], v[148:151]\n\t"
      "v_mfma_f32_16x16x32_bf16 v[144:147], a[68:71], a[20:23], v[144:147]\n\t"
      "v_mfma_f32_16x16x32_bf16 v[140:143], a[64:67], a[20:23], v[140:143]\n\t"
      "v_mfma_f32_16x16x32_bf16 v[168:171], a[64:67], a[24:27], v[168:171]\n\t"
      "v_mfma_f32_16x16x32_bf16 v[172:175], a[68:71], a[24:27], v[172:175]\n\t"
      "v_mfma_f32_16x16x32_bf16 v[176:179], a[72:75], a[24:27], v[176:179]\n\t"
      "v_mfma_f32_16x16x32_bf16 v[180:183], a[76:79], a[24:27], v[180:183]\n\t"
      "v_mfma_f32_16x16x32_bf16 v[184:187], a[80:83], a[24:27], v[184:187]\n\t"
      "v_mfma_f32_16x16x32_bf16 v[188:191], a[84:87], a[24:27], v[188:191]\n\t"
      "v_mfma_f32_16x16x32_bf16 v[192:195], a[88:91], a[24:27], v[192:195]\n\t"
      "v_mfma_f32_16x16x32_bf16 v[220:223], a[88:91], a[28:31], v[220:223]\n\t"
      "v_mfma_f32_16x16x32_bf16 v[216:219], a[84:87], a[28:31], v[216:219]\n\t"
      "v_mfma_f32_16x16x32_bf16 v[212:215], a[80:83], a[28:31], v[212:215]\n\t"
      "v_mfma_f32_16x16x32_bf16 v[208:211], a[76:79], a[28:31], v[208:211]\n\t"
      "v_mfma_f32_16x16x32_bf16 v[204:207], a[72:75], a[28:31], v[204:207]\n\t"
      "v_mfma_f32_16x16x32_bf16 v[200:203], a[68:71], a[28:31], v[200:203]\n\t"
      "v_mfma_f32_16x16x32_bf16 v[196:199], a[64:67], a[28:31], v[196:199]\n\t"
      "v_mfma_f32_16x16x32_bf16 v[0:3], a[92:95], a[32:35], v[0:3]\n\t"
      "v_mfma_f32_16x16x32_bf16 v[4:7], a[96:99], a[32:35], v[4:7]\n\t"
      "v_mfma_f32_16x16x32_bf16 v[8:11], a[100:103], a[32:35], v[8:11]\n\t"
      "v_mfma_f32_16x16x32_bf16 v[12:15], a[104:107], a[32:35], v[12:15]\n\t"
      "v_mfma_f32_16x16x32_bf16 v[16:19], a[108:111], a[32:35], v[16:19]\n\t"
      "v_mfma_f32_16x16x32_bf16 v[20:23], a[112:115], a[32:35], v[20:23]\n\t"
      "v_mfma_f32_16x16x32_bf16 v[24:27], a[116:119], a[32:35], v[24:27]\n\t"
      "v_mfma_f32_16x16x32_bf16 v[52:55], a[116:119], a[36:39], v[52:55]\n\t"
      "v_mfma_f32_16x16x32_bf16 v[48:51], a[112:115], a[36:39], v[48:51]\n\t"
      "v_mfma_f32_16x16x32_bf16 v[44:47], a[108:111], a[36:39], v[44:47]\n\t"
      "v_mfma_f32_16x16x32_bf16 v[40:43], a[104:107], a[36:39], v[40:43]\n\t"
      "v_mfma_f32_16x16x32_bf16 v[36:39], a[100:103], a[36:39], v[36:39]\n\t"
      "v_mfma_f32_16x16x32_bf16 v[32:35], a[96:99], a[36:39], v[32:35]\n\t"
      "v_mfma_f32_16x16x32_bf16 v[28:31], a[92:95], a[36:39], v[28:31]\n\t"
      "v_mfma_f32_16x16x32_bf16 v[56:59], a[92:95], a[40:43], v[56:59]\n\t"
      "v_mfma_f32_16x16x32_bf16 v[60:63], a[96:99], a[40:43], v[60:63]\n\t"
      "v_mfma_f32_16x16x32_bf16 v[64:67], a[100:103], a[40:43], v[64:67]\n\t"
      "v_mfma_f32_16x16x32_bf16 v[68:71], a[104:107], a[40:43], v[68:71]\n\t"
      "v_mfma_f32_16x16x32_bf16 v[72:75], a[108:111], a[40:43], v[72:75]\n\t"
      "v_mfma_f32_16x16x32_bf16 v[76:79], a[112:115], a[40:43], v[76:79]\n\t"
      "v_mfma_f32_16x16x32_bf16 v[80:83], a[116:119], a[40:43], v[80:83]\n\t"
      "ds_read_b64_tr_b16 a[64:65], v226 offset:0\n\t"
      "v_mfma_f32_16x16x32_bf16 v[108:111], a[116:119], a[44:47], v[108:111]\n\t"
      "ds_read_b64_tr_b16 a[66:67], v226 offset:896\n\t"
      "v_mfma_f32_16x16x32_bf16 v[104:107], a[112:115], a[44:47], v[104:107]\n\t"
      "ds_read_b64_tr_b16 a[68:69], v227 offset:0\n\t"
      "v_mfma_f32_16x16x32_bf16 v[100:103], a[108:111], a[44:47], v[100:103]\n\t"
      "ds_read_b64_tr_b16 a[70:71], v227 offset:896\n\t"
      "v_mfma_f32_16x16x32_bf16 v[96:99], a[104:107], a[44:47], v[96:99]\n\t"
      "ds_read_b64_tr_b16 a[72:73], v226 offset:32\n\t"
      "v_mfma_f32_16x16x32_bf16 v[92:95], a[100:103], a[44:47], v[92:95]\n\t"
      "ds_read_b64_tr_b16 a[74:75], v226 offset:928\n\t"
      "v_mfma_f32_16x16x32_bf16 v[88:91], a[96:99], a[44:47], v[88:91]\n\t"
      "ds_read_b64_tr_b16 a[76:77], v227 offset:32\n\t"
      "v_mfma_f32_16x16x32_bf16 v[84:87], a[92:95], a[44:47], v[84:87]\n\t"
      "ds_read_b64_tr_b16 a[78:79], v227 offset:928\n\t"
      "v_mfma_f32_16x16x32_bf16 v[112:115], a[92:95], a[48:51], v[112:115]\n\t"
      "ds_read_b64_tr_b16 a[80:81], v226 offset:64\n\t"
      "v_mfma_f32_16x16x32_bf16 v[116:119], a[96:99], a[48:51], v[116:119]\n\t"
      "ds_read_b64_tr_b16 a[82:83], v226 offset:960\n\t"
      "v_mfma_f32_16x16x32_bf16 v[120:123], a[100:103], a[48:51], v[120:123]\n\t"
      "ds_read_b64_tr_b16 a[84:85], v227 offset:64\n\t"
      "v_mfma_f32_16x16x32_bf16 v[124:127], a[104:107], a[48:51], v[124:127]\n\t"
      "ds_read_b64_tr_b16 a[86:87], v227 offset:960\n\t"
      "v_mfma_f32_16x16x32_bf16 v[128:131], a[108:111], a[48:51], v[128:131]\n\t"
      "ds_read_b64_tr_b16 a[88:89], v228 offset:0\n\t"
      "v_mfma_f32_16x16x32_bf16 v[132:135], a[112:115], a[48:51], v[132:135]\n\t"
      "ds_read_b64_tr_b16 a[90:91], v228 offset:896\n\t"
      "v_mfma_f32_16x16x32_bf16 v[136:139], a[116:119], a[48:51], v[136:139]\n\t"
      "ds_read_b128 a[0:3], v224 offset:0\n\t"
      "v_mfma_f32_16x16x32_bf16 v[164:167], a[116:119], a[52:55], v[164:167]\n\t"
      "ds_read_b128 a[4:7], v224 offset:2048\n\t"
      "v_mfma_f32_16x16x32_bf16 v[160:163], a[112:115], a[52:55], v[160:163]\n\t"
      "ds_read_b128 a[8:11], v224 offset:4096\n\t"
      "v_mfma_f32_16x16x32_bf16 v[156:159], a[108:111], a[52:55], v[156:159]\n\t"
      "ds_read_b128 a[12:15], v224 offset:6144\n\t"
      "v_mfma_f32_16x16x32_bf16 v[152:155], a[104:107], a[52:55], v[152:155]\n\t"
      "ds_read_b128 a[16:19], v224 offset:8192\n\t"
      "v_mfma_f32_16x16x32_bf16 v[148:151], a[100:103], a[52:55], v[148:151]\n\t"
      "ds_read_b128 a[20:23], v224 offset:10240\n\t"
      "v_mfma_f32_16x16x32_bf16 v[144:147], a[96:99], a[52:55], v[144:147]\n\t"
      "ds_read_b128 a[24:27], v224 offset:12288\n\t"
      "v_mfma_f32_16x16x32_bf16 v[140:143], a[92:95], a[52:55], v[140:143]\n\t"
      "ds_read_b128 a[28:31], v224 offset:14336\n\t"
      "v_mfma_f32_16x16x32_bf16 v[168:171], a[92:95], a[56:59], v[168:171]\n\t"
      "v_mfma_f32_16x16x32_bf16 v[172:175], a[96:99], a[56:59], v[172:175]\n\t"
      "v_mfma_f32_16x16x32_bf16 v[176:179], a[100:103], a[56:59], v[176:179]\n\t"
      "v_mfma_f32_16x16x32_bf16 v[180:183], a[104:107], a[56:59], v[180:183]\n\t"
      "v_mfma_f32_16x16x32_bf16 v[184:187], a[108:111], a[56:59], v[184:187]\n\t"
      "v_mfma_f32_16x16x32_bf16 v[188:191], a[112:115], a[56:59], v[188:191]\n\t"
      "v_mfma_f32_16x16x32_bf16 v[192:195], a[116:119], a[56:59], v[192:195]\n\t"
      "v_mfma_f32_16x16x32_bf16 v[220:223], a[116:119], a[60:63], v[220:223]\n\t"
      "v_mfma_f32_16x16x32_bf16 v[216:219], a[112:115], a[60:63], v[216:219]\n\t"
      "v_mfma_f32_16x16x32_bf16 v[212:215], a[108:111], a[60:63], v[212:215]\n\t"
      "v_mfma_f32_16x16x32_bf16 v[208:211], a[104:107], a[60:63], v[208:211]\n\t"
      "s_sub_u32 s47, 0, s47\n\t"
      "v_mfma_f32_16x16x32_bf16 v[204:207], a[100:103], a[60:63], v[204:207]\n\t"
      "v_mfma_f32_16x16x32_bf16 v[200:203], a[96:99], a[60:63], v[200:203]\n\t"
      "s_waitcnt lgkmcnt(0)\n\t"
      "v_mfma_f32_16x16x32_bf16 v[196:199], a[92:95], a[60:63], v[196:199]\n\t"
      "9:\n\t"
      "v_mfma_f32_16x16x32_bf16 v[0:3], a[64:67], a[0:3], v[0:3]\n\t"
      "ds_read_b64_tr_b16 a[92:93], v226 offset:7168\n\t"
      "ds_read_b64_tr_b16 a[94:95], v226 offset:8064\n\t"
      "v_mfma_f32_16x16x32_bf16 v[4:7], a[68:71], a[0:3], v[4:7]\n\t"
      "ds_read_b64_tr_b16 a[96:97], v227 offset:7168\n\t"
      "ds_read_b64_tr_b16 a[98:99], v227 offset:8064\n\t"
      "v_mfma_f32_16x16x32_bf16 v[8:11], a[72:75], a[0:3], v[8:11]\n\t"
      "ds_read_b64_tr_b16 a[100:101], v226 offset:7200\n\t"
      "ds_read_b64_tr_b16 a[102:103], v226 offset:8096\n\t"
      "v_mfma_f32_16x16x32_bf16 v[12:15], a[76:79], a[0:3], v[12:15]\n\t"
      "ds_read_b64_tr_b16 a[104:105], v227 offset:7200\n\t"
      "ds_read_b64_tr_b16 a[106:107], v227 offset:8096\n\t"
      "v_mfma_f32_16x16x32_bf16 v[16:19], a[80:83], a[0:3], v[16:19]\n\t"
      "ds_read_b64_tr_b16 a[108:109], v226 offset:7232\n\t"
      "ds_read_b64_tr_b16 a[110:111], v226 offset:8128\n\t"
      "v_mfma_f32_16x16x32_bf16 v[20:23], a[84:87], a[0:3], v[20:23]\n\t"
      "ds_read_b64_tr_b16 a[112:113], v227 offset:7232\n\t"
      "ds_read_b64_tr_b16 a[114:115], v227 offset:8128\n\t"
      "v_mfma_f32_16x16x32_bf16 v[24:27], a[88:91], a[0:3], v[24:27]\n\t"
      "ds_read_b64_tr_b16 a[116:117], v228 offset:7168\n\t"
      "ds_read_b64_tr_b16 a[118:119], v228 offset:8064\n\t"
      "v_mfma_f32_16x16x32_bf16 v[52:55], a[88:91], a[4:7], v[52:55]\n\t"
      "ds_read_b128 a[32:35], v225 offset:0\n\t"
      "v_mfma_f32_16x16x32_bf16 v[48:51], a[84:87], a[4:7], v[48:51]\n\t"
      "ds_read_b128 a[36:39], v225 offset:2048\n\t"
      "v_mfma_f32_16x16x32_bf16 v[44:47], a[80:83], a[4:7], v[44:47]\n\t"
      "ds_read_b128 a[40:43], v225 offset:4096\n\t"
      "v_mfma_f32_16x16x32_bf16 v[40:43], a[76:79], a[4:7], v[40:43]\n\t"
      "ds_read_b128 a[44:47], v225 offset:6144\n\t"
      "v_mfma_f32_16x16x32_bf16 v[36:39], a[72:75], a[4:7], v[36:39]\n\t"
      "ds_read_b128 a[48:51], v225 offset:8192\n\t"
      "v_mfma_f32_16x16x32_bf16 v[32:35], a[68:71], a[4:7], v[32:35]\n\t"
      "ds_read_b128 a[52:55], v225 offset:10240\n\t"
      "v_mfma_f32_16x16x32_bf16 v[28:31], a[64:67], a[4:7], v[28:31]\n\t"
      "ds_read_b128 a[56:59], v225 offset:12288\n\t"
      "v_mfma_f32_16x16x32_bf16 v[56:59], a[64:67], a[8:11], v[56:59]\n\t"
      "ds_read_b128 a[60:63], v225 offset:14336\n\t"
      "v_mfma_f32_16x16x32_bf16 v[60:63], a[68:71], a[8:11], v[60:63]\n\t"
      "v_mfma_f32_16x16x32_bf16 v[64:67], a[72:75], a[8:11], v[64:67]\n\t"
      "v_mov_b32 v248, 0xc0135761\n\t"
      "v_mov_b32 v249, 0xc0135761\n\t"
      "v_mfma_f32_16x16x32_bf16 v[68:71], a[76:79], a[8:11], v[68:71]\n\t"
      "s_mov_b32 s54, 0xbdd2d3e8\n\t"
      "s_mov_b32 s55, 0xbdd2d3e8\n\t"
      "v_mfma_f32_16x16x32_bf16 v[72:75], a[80:83], a[8:11], v[72:75]\n\t"
      "s_mov_b32 s64, 0\n\t"
      "v_mfma_f32_16x16x32_bf16 v[76:79], a[84:87], a[8:11], v[76:79]\n\t"
      "s_mov_b32 s65, 229376\n\t"
      "v_mfma_f32_16x16x32_bf16 v[80:83], a[88:91], a[8:11], v[80:83]\n\t"
      "s_mov_b32 s66, 458752\n\t"
      "v_mfma_f32_16x16x32_bf16 v[108:111], a[88:91], a[12:15], v[108:111]\n\t"
      "s_mov_b32 s67, 688128\n\t"
      "v_mfma_f32_16x16x32_bf16 v[104:107], a[84:87], a[12:15], v[104:107]\n\t"
      "v_mfma_f32_16x16x32_bf16 v[100:103], a[80:83], a[12:15], v[100:103]\n\t"
      "v_mfma_f32_16x16x32_bf16 v[96:99], a[76:79], a[12:15], v[96:99]\n\t"
      "v_mfma_f32_16x16x32_bf16 v[92:95], a[72:75], a[12:15], v[92:95]\n\t"
      "v_mfma_f32_16x16x32_bf16 v[88:91], a[68:71], a[12:15], v[88:91]\n\t"
      "s_waitcnt lgkmcnt(0)\n\t"
      "s_barrier\n\t"
      "v_mfma_f32_16x16x32_bf16 v[84:87], a[64:67], a[12:15], v[84:87]\n\t"
      "s_add_u32 m0, %[ldsW2], 0\n\t"
      "v_mfma_f32_16x16x32_bf16 v[0:3], a[92:95], a[32:35], v[0:3]\n\t"
      "s_mov_b32 s70, 0\n\t"
      "v_mfma_f32_16x16x32_bf16 v[4:7], a[96:99], a[32:35], v[4:7]\n\t"
      "buffer_load_dwordx4 v254, %[srdW2], s70 offen offset:0 lds\n\t"
      "v_mfma_f32_16x16x32_bf16 v[8:11], a[100:103], a[32:35], v[8:11]\n\t"
      "s_mov_b32 s70, 7168\n\t"
      "v_mfma_f32_16x16x32_bf16 v[12:15], a[104:107], a[32:35], v[12:15]\n\t"
      "buffer_load_dwordx4 v254, %[srdW2], s70 offen offset:1024 lds\n\t"
      "v_mfma_f32_16x16x32_bf16 v[16:19], a[108:111], a[32:35], v[16:19]\n\t"
      "s_mov_b32 s70, 14336\n\t"
      "v_mfma_f32_16x16x32_bf16 v[20:23], a[112:115], a[32:35], v[20:23]\n\t"
      "buffer_load_dwordx4 v255, %[srdW2], s70 offen offset:2048 lds\n\t"
      "v_mfma_f32_16x16x32_bf16 v[24:27], a[116:119], a[32:35], v[24:27]\n\t"
      "s_mov_b32 s70, 21504\n\t"
      "v_mfma_f32_16x16x32_bf16 v[52:55], a[116:119], a[36:39], v[52:55]\n\t"
      "buffer_load_dwordx4 v255, %[srdW2], s70 offen offset:3072 lds\n\t"
      "v_mfma_f32_16x16x32_bf16 v[48:51], a[112:115], a[36:39], v[48:51]\n\t"
      "s_add_u32 m0, %[ldsW2], 32768\n\t"
      "v_mfma_f32_16x16x32_bf16 v[44:47], a[108:111], a[36:39], v[44:47]\n\t"
      "s_mov_b32 s70, 131072\n\t"
      "v_mfma_f32_16x16x32_bf16 v[40:43], a[104:107], a[36:39], v[40:43]\n\t"
      "buffer_load_dwordx4 v254, %[srdW2], s70 offen offset:0 lds\n\t"
      "v_mfma_f32_16x16x32_bf16 v[36:39], a[100:103], a[36:39], v[36:39]\n\t"
      "s_mov_b32 s70, 138240\n\t"
      "v_mfma_f32_16x16x32_bf16 v[32:35], a[96:99], a[36:39], v[32:35]\n\t"
      "buffer_load_dwordx4 v254, %[srdW2], s70 offen offset:1024 lds\n\t"
      "v_mfma_f32_16x16x32_bf16 v[28:31], a[92:95], a[36:39], v[28:31]\n\t"
      "s_mov_b32 s70, 145408\n\t"
      "v_mfma_f32_16x16x32_bf16 v[56:59], a[92:95], a[40:43], v[56:59]\n\t"
      "buffer_load_dwordx4 v255, %[srdW2], s70 offen offset:2048 lds\n\t"
      "v_mfma_f32_16x16x32_bf16 v[60:63], a[96:99], a[40:43], v[60:63]\n\t"
      "s_mov_b32 s70, 152576\n\t"
      "v_mfma_f32_16x16x32_bf16 v[64:67], a[100:103], a[40:43], v[64:67]\n\t"
      "buffer_load_dwordx4 v255, %[srdW2], s70 offen offset:3072 lds\n\t"
      "v_mfma_f32_16x16x32_bf16 v[68:71], a[104:107], a[40:43], v[68:71]\n\t"
      "s_add_u32 m0, %[ldsW2], 65536\n\t"
      "v_mfma_f32_16x16x32_bf16 v[72:75], a[108:111], a[40:43], v[72:75]\n\t"
      "s_mov_b32 s70, 262144\n\t"
      "v_mfma_f32_16x16x32_bf16 v[76:79], a[112:115], a[40:43], v[76:79]\n\t"
      "buffer_load_dwordx4 v254, %[srdW2], s70 offen offset:0 lds\n\t"
      "v_mfma_f32_16x16x32_bf16 v[80:83], a[116:119], a[40:43], v[80:83]\n\t"
      "s_mov_b32 s70, 269312\n\t"
      "v_mfma_f32_16x16x32_bf16 v[108:111], a[116:119], a[44:47], v[108:111]\n\t"
      "buffer_load_dwordx4 v254, %[srdW2], s70 offen offset:1024 lds\n\t"
      "v_permlane32_swap_b32 v0, v28\n\t"
      "v_permlane32_swap_b32 v1, v29\n\t"
      "v_mfma_f32_16x16x32_bf16 v[104:107], a[112:115], a[44:47], v[104:107]\n\t"
      "s_mov_b32 s70, 276480\n\t"
      "v_permlane32_swap_b32 v2, v30\n\t"
      "v_permlane32_swap_b32 v3, v31\n\t"
      "v_mfma_f32_16x16x32_bf16 v[100:103], a[108:111], a[44:47], v[100:103]\n\t"
      "buffer_load_dwordx4 v255, %[srdW2], s70 offen offset:2048 lds\n\t"
      "s_nop 0\n\t"
      "v_pk_mul_f32 v[232:233], v[0:1], v[0:1]\n\t"
      "v_mfma_f32_16x16x32_bf16 v[96:99], a[104:107], a[44:47], v[96:99]\n\t"
      "s_mov_b32 s70, 283648\n\t"
      "v_pk_mul_f32 v[234:235], v[2:3], v[2:3]\n\t"
      "v_pk_fma_f32 v[232:233], v[232:233], s[54:55], v[248:249]\n\t"
      "v_mfma_f32_16x16x32_bf16 v[92:95], a[100:103], a[44:47], v[92:95]\n\t"
      "buffer_load_dwordx4 v255, %[srdW2], s70 offen offset:3072 lds\n\t"
      "v_pk_fma_f32 v[234:235], v[234:235], s[54:55], v[248:249]\n\t"
      "v_pk_mul_f32 v[232:233], v[0:1], v[232:233]\n\t"
      "v_mfma_f32_16x16x32_bf16 v[88:91], a[96:99], a[44:47], v[88:91]\n\t"
      "s_add_u32 m0, %[ldsW2], 98304\n\t"
      "v_pk_mul_f32 v[234:235], v[2:3], v[234:235]\n\t"
      "v_mfma_f32_16x16x32_bf16 v[84:87], a[92:95], a[44:47], v[84:87]\n\t"
      "s_mov_b32 s70, 393216\n\t"
      "v_exp_f32 v232, v232\n\t"
      "v_mfma_f32_16x16x32_bf16 v[112:115], a[64:67], a[16:19], v[112:115]\n\t"
      "buffer_load_dwordx4 v254, %[srdW2], s70 offen offset:0 lds\n\t"
      "v_exp_f32 v233, v233\n\t"
      "v_mfma_f32_16x16x32_bf16 v[116:119], a[68:71], a[16:19], v[116:119]\n\t"
      "s_mov_b32 s70, 400384\n\t"
      "v_exp_f32 v234, v234\n\t"
      "v_mfma_f32_16x16x32_bf16 v[120:123], a[72:75], a[16:19], v[120:123]\n\t"
      "buffer_load_dwordx4 v254, %[srdW2], s70 offen offset:1024 lds\n\t"
      "v_exp_f32 v235, v235\n\t"
      "v_mfma_f32_16x16x32_bf16 v[124:127], a[76:79], a[16:19], v[124:127]\n\t"
      "s_mov_b32 s70, 407552\n\t"
      "v_pk_mul_f32 v[0:1], v[0:1], v[28:29]\n\t"
      "v_pk_mul_f32 v[2:3], v[2:3], v[30:31]\n\t"
      "v_mfma_f32_16x16x32_bf16 v[128:131], a[80:83], a[16:19], v[128:131]\n\t"
      "buffer_load_dwordx4 v255, %[srdW2], s70 offen offset:2048 lds\n\t"
      "v_pk_add_f32 v[232:233], v[232:233], 1.0 op_sel_hi:[1,0]\n\t"
      "v_pk_add_f32 v[234:235], v[234:235], 1.0 op_sel_hi:[1,0]\n\t"
      "v_mfma_f32_16x16x32_bf16 v[132:135], a[84:87], a[16:19], v[132:135]\n\t"
      "s_mov_b32 s70, 414720\n\t"
      "v_rcp_f32 v232, v232\n\t"
      "v_mfma_f32_16x16x32_bf16 v[136:139], a[88:91], a[16:19], v[136:139]\n\t"
      "buffer_load_dwordx4 v255, %[srdW2], s70 offen offset:3072 lds\n\t"
      "v_rcp_f32 v233, v233\n\t"
      "v_mfma_f32_16x16x32_bf16 v[164:167], a[88:91], a[20:23], v[164:167]\n\t"
      "v_rcp_f32 v234, v234\n\t"
      "v_mfma_f32_16x16x32_bf16 v[160:163], a[84:87], a[20:23], v[160:163]\n\t"
      "v_rcp_f32 v235, v235\n\t"
      "v_mfma_f32_16x16x32_bf16 v[156:159], a[80:83], a[20:23], v[156:159]\n\t"
      "s_nop 0\n\t"
      "v_pk_mul_f32 v[0:1], v[0:1], v[232:233]\n\t"
      "v_mfma_f32_16x16x32_bf16 v[152:155], a[76:79], a[20:23], v[152:155]\n\t"
      "v_pk_mul_f32 v[2:3], v[2:3], v[234:235]\n\t"
      "v_cvt_pk_bf16_f32 v240, v0, v1\n\t"
      "v_mfma_f32_16x16x32_bf16 v[148:151], a[72:75], a[20:23], v[148:151]\n\t"
      "v_cvt_pk_bf16_f32 v241, v2, v3\n\t"
      "v_permlane32_swap_b32 v4, v32\n\t"
      "v_mfma_f32_16x16x32_bf16 v[144:147], a[68:71], a[20:23], v[144:147]\n\t"
      "v_permlane32_swap_b32 v5, v33\n\t"
      "v_permlane32_swap_b32 v6, v34\n\t"
      "v_mfma_f32_16x16x32_bf16 v[140:143], a[64:67], a[20:23], v[140:143]\n\t"
      "v_permlane32_swap_b32 v7, v35\n\t"
      "s_nop 0\n\t"
      "v_mfma_f32_16x16x32_bf16 v[112:115], a[92:95], a[48:51], v[112:115]\n\t"
      "v_pk_mul_f32 v[236:237], v[4:5], v[4:5]\n\t"
      "v_pk_mul_f32 v[238:239], v[6:7], v[6:7]\n\t"
      "v_mfma_f32_16x16x32_bf16 v[116:119], a[96:99], a[48:51], v[116:119]\n\t"
      "v_pk_fma_f32 v[236:237], v[236:237], s[54:55], v[248:249]\n\t"
      "v_pk_fma_f32 v[238:239], v[238:239], s[54:55], v[248:249]\n\t"
      "v_mfma_f32_16x16x32_bf16 v[120:123], a[100:103], a[48:51], v[120:123]\n\t"
      "v_pk_mul_f32 v[236:237], v[4:5], v[236:237]\n\t"
      "v_pk_mul_f32 v[238:239], v[6:7], v[238:239]\n\t"
      "v_mfma_f32_16x16x32_bf16 v[124:127], a[104:107], a[48:51], v[124:127]\n\t"
      "v_exp_f32 v236, v236\n\t"
      "v_mfma_f32_16x16x32_bf16 v[128:131], a[108:111], a[48:51], v[128:131]\n\t"
      "v_exp_f32 v237, v237\n\t"
      "v_mfma_f32_16x16x32_bf16 v[132:135], a[112:115], a[48:51], v[132:135]\n\t"
      "v_exp_f32 v238, v238\n\t"
      "v_mfma_f32_16x16x32_bf16 v[136:139], a[116:119], a[48:51], v[136:139]\n\t"
      "v_exp_f32 v239, v239\n\t"
      "v_mfma_f32_16x16x32_bf16 v[164:167], a[116:119], a[52:55], v[164:167]\n\t"
      "v_pk_mul_f32 v[4:5], v[4:5], v[32:33]\n\t"
      "v_pk_mul_f32 v[6:7], v[6:7], v[34:35]\n\t"
      "v_mfma_f32_16x16x32_bf16 v[160:163], a[112:115], a[52:55], v[160:163]\n\t"
      "v_pk_add_f32 v[236:237], v[236:237], 1.0 op_sel_hi:[1,0]\n\t"
      "v_pk_add_f32 v[238:239], v[238:239], 1.0 op_sel_hi:[1,0]\n\t"
      "v_mfma_f32_16x16x32_bf16 v[156:159], a[108:111], a[52:55], v[156:159]\n\t"
      "v_rcp_f32 v236, v236\n\t"
      "v_mfma_f32_16x16x32_bf16 v[152:155], a[104:107], a[52:55], v[152:155]\n\t"
      "v_rcp_f32 v237, v237\n\t"
      "v_mfma_f32_16x16x32_bf16 v[148:151], a[100:103], a[52:55], v[148:151]\n\t"
      "v_rcp_f32 v238, v238\n\t"
      "v_mfma_f32_16x16x32_bf16 v[144:147], a[96:99], a[52:55], v[144:147]\n\t"
      "v_rcp_f32 v239, v239\n\t"
      "v_mfma_f32_16x16x32_bf16 v[140:143], a[92:95], a[52:55], v[140:143]\n\t"
      "s_nop 0\n\t"
      "v_pk_mul_f32 v[4:5], v[4:5], v[236:237]\n\t"
      "v_mfma_f32_16x16x32_bf16 v[168:171], a[64:67], a[24:27], v[168:171]\n\t"
      "v_pk_mul_f32 v[6:7], v[6:7], v[238:239]\n\t"
      "v_cvt_pk_bf16_f32 v242, v4, v5\n\t"
      "v_mfma_f32_16x16x32_bf16 v[172:175], a[68:71], a[24:27], v[172:175]\n\t"
      "v_cvt_pk_bf16_f32 v243, v6, v7\n\t"
      "v_cndmask_b32 v244, v240, v242, s[58:59]\n\t"
      "v_mfma_f32_16x16x32_bf16 v[176:179], a[72:75], a[24:27], v[176:179]\n\t"
      "v_cndmask_b32 v245, v241, v243, s[58:59]\n\t"
      "v_cndmask_b32 v246, v242, v240, s[58:59]\n\t"
      "v_mfma_f32_16x16x32_bf16 v[180:183], a[76:79], a[24:27], v[180:183]\n\t"
      "v_cndmask_b32 v247, v243, v241, s[58:59]\n\t"
      "v_mfma_f32_16x16x32_bf16 v[184:187], a[80:83], a[24:27], v[184:187]\n\t"
      "buffer_store_dwordx4 v[244:247], v230, %[srdH], s64 offen offset:0 sc1\n\t"
      "v_mfma_f32_16x16x32_bf16 v[188:191], a[84:87], a[24:27], v[188:191]\n\t"
      "v_permlane32_swap_b32 v8, v36\n\t"
      "v_permlane32_swap_b32 v9, v37\n\t"
      "v_mfma_f32_16x16x32_bf16 v[192:195], a[88:91], a[24:27], v[192:195]\n\t"
      "v_permlane32_swap_b32 v10, v38\n\t"
      "v_permlane32_swap_b32 v11, v39\n\t"
      "v_mfma_f32_16x16x32_bf16 v[220:223], a[88:91], a[28:31], v[220:223]\n\t"
      "s_nop 0\n\t"
      "v_pk_mul_f32 v[232:233], v[8:9], v[8:9]\n\t"
      "v_mfma_f32_16x16x32_bf16 v[216:219], a[84:87], a[28:31], v[216:219]\n\t"
      "v_pk_mul_f32 v[234:235], v[10:11], v[10:11]\n\t"
      "v_pk_fma_f32 v[232:233], v[232:233], s[54:55], v[248:249]\n\t"
      "v_mfma_f32_16x16x32_bf16 v[212:215], a[80:83], a[28:31], v[212:215]\n\t"
      "v_pk_fma_f32 v[234:235], v[234:235], s[54:55], v[248:249]\n\t"
      "v_pk_mul_f32 v[232:233], v[8:9], v[232:233]\n\t"
      "v_mfma_f32_16x16x32_bf16 v[208:211], a[76:79], a[28:31], v[208:211]\n\t"
      "v_pk_mul_f32 v[234:235], v[10:11], v[234:235]\n\t"
      "v_mfma_f32_16x16x32_bf16 v[204:207], a[72:75], a[28:31], v[204:207]\n\t"
      "v_exp_f32 v232, v232\n\t"
      "v_mfma_f32_16x16x32_bf16 v[200:203], a[68:71], a[28:31], v[200:203]\n\t"
      "v_exp_f32 v233, v233\n\t"
      "v_mfma_f32_16x16x32_bf16 v[196:199], a[64:67], a[28:31], v[196:199]\n\t"
      "v_exp_f32 v234, v234\n\t"
      "v_mfma_f32_16x16x32_bf16 v[168:171], a[92:95], a[56:59], v[168:171]\n\t"
      "v_exp_f32 v235, v235\n\t"
      "v_mfma_f32_16x16x32_bf16 v[172:175], a[96:99], a[56:59], v[172:175]\n\t"
      "v_pk_mul_f32 v[8:9], v[8:9], v[36:37]\n\t"
      "v_pk_mul_f32 v[10:11], v[10:11], v[38:39]\n\t"
      "v_mfma_f32_16x16x32_bf16 v[176:179], a[100:103], a[56:59], v[176:179]\n\t"
      "v_pk_add_f32 v[232:233], v[232:233], 1.0 op_sel_hi:[1,0]\n\t"
      "v_pk_add_f32 v[234:235], v[234:235], 1.0 op_sel_hi:[1,0]\n\t"
      "v_mfma_f32_16x16x32_bf16 v[180:183], a[104:107], a[56:59], v[180:183]\n\t"
      "v_rcp_f32 v232, v232\n\t"
      "v_mfma_f32_16x16x32_bf16 v[184:187], a[108:111], a[56:59], v[184:187]\n\t"
      "v_rcp_f32 v233, v233\n\t"
      "v_mfma_f32_16x16x32_bf16 v[188:191], a[112:115], a[56:59], v[188:191]\n\t"
      "v_rcp_f32 v234, v234\n\t"
      "v_mfma_f32_16x16x32_bf16 v[192:195], a[116:119], a[56:59], v[192:195]\n\t"
      "v_rcp_f32 v235, v235\n\t"
      "v_mfma_f32_16x16x32_bf16 v[220:223], a[116:119], a[60:63], v[220:223]\n\t"
      "s_nop 0\n\t"
      "v_pk_mul_f32 v[8:9], v[8:9], v[232:233]\n\t"
      "v_mfma_f32_16x16x32_bf16 v[216:219], a[112:115], a[60:63], v[216:219]\n\t"
      "v_pk_mul_f32 v[10:11], v[10:11], v[234:235]\n\t"
      "v_cvt_pk_bf16_f32 v240, v8, v9\n\t"
      "v_mfma_f32_16x16x32_bf16 v[212:215], a[108:111], a[60:63], v[212:215]\n\t"
      "v_cvt_pk_bf16_f32 v241, v10, v11\n\t"
      "v_permlane32_swap_b32 v12, v40\n\t"
      "v_mfma_f32_16x16x32_bf16 v[208:211], a[104:107], a[60:63], v[208:211]\n\t"
      "v_permlane32_swap_b32 v13, v41\n\t"
      "v_permlane32_swap_b32 v14, v42\n\t"
      "v_mfma_f32_16x16x32_bf16 v[204:207], a[100:103], a[60:63], v[204:207]\n\t"
      "v_permlane32_swap_b32 v15, v43\n\t"
      "s_nop 0\n\t"
      "v_mfma_f32_16x16x32_bf16 v[200:203], a[96:99], a[60:63], v[200:203]\n\t"
      "v_pk_mul_f32 v[236:237], v[12:13], v[12:13]\n\t"
      "v_pk_mul_f32 v[238:239], v[14:15], v[14:15]\n\t"
      "v_mfma_f32_16x16x32_bf16 v[196:199], a[92:95], a[60:63], v[196:199]\n\t"
      "v_pk_fma_f32 v[236:237], v[236:237], s[54:55], v[248:249]\n\t"
      "v_pk_fma_f32 v[238:239], v[238:239], s[54:55], v[248:249]\n\t"
      "s_nop 7\n\t"
      "v_pk_mul_f32 v[236:237], v[12:13], v[236:237]\n\t"
      "v_pk_mul_f32 v[238:239], v[14:15], v[238:239]\n\t"
      "v_exp_f32 v236, v236\n\t"
      "v_exp_f32 v237, v237\n\t"
      "v_exp_f32 v238, v238\n\t"
      "v_exp_f32 v239, v239\n\t"
      "v_pk_mul_f32 v[12:13], v[12:13], v[40:41]\n\t"
      "v_pk_mul_f32 v[14:15], v[14:15], v[42:43]\n\t"
      "v_pk_add_f32 v[236:237], v[236:237], 1.0 op_sel_hi:[1,0]\n\t"
      "v_pk_add_f32 v[238:239], v[238:239], 1.0 op_sel_hi:[1,0]\n\t"
      "v_rcp_f32 v236, v236\n\t"
      "v_rcp_f32 v237, v237\n\t"
      "v_rcp_f32 v238, v238\n\t"
      "v_rcp_f32 v239, v239\n\t"
      "s_nop 0\n\t"
      "v_pk_mul_f32 v[12:13], v[12:13], v[236:237]\n\t"
      "v_pk_mul_f32 v[14:15], v[14:15], v[238:239]\n\t"
      "v_cvt_pk_bf16_f32 v242, v12, v13\n\t"
      "v_cvt_pk_bf16_f32 v243, v14, v15\n\t"
      "v_cndmask_b32 v244, v240, v242, s[58:59]\n\t"
      "v_cndmask_b32 v245, v241, v243, s[58:59]\n\t"
      "v_cndmask_b32 v246, v242, v240, s[58:59]\n\t"
      "v_cndmask_b32 v247, v243, v241, s[58:59]\n\t"
      "buffer_store_dwordx4 v[244:247], v230, %[srdH], s64 offen offset:512 sc1\n\t"
      "v_permlane32_swap_b32 v16, v44\n\t"
      "v_permlane32_swap_b32 v17, v45\n\t"
      "v_permlane32_swap_b32 v18, v46\n\t"
      "v_permlane32_swap_b32 v19, v47\n\t"
      "s_nop 0\n\t"
      "v_pk_mul_f32 v[232:233], v[16:17], v[16:17]\n\t"
      "v_pk_mul_f32 v[234:235], v[18:19], v[18:19]\n\t"
      "v_pk_fma_f32 v[232:233], v[232:233], s[54:55], v[248:249]\n\t"
      "v_pk_fma_f32 v[234:235], v[234:235], s[54:55], v[248:249]\n\t"
      "v_pk_mul_f32 v[232:233], v[16:17], v[232:233]\n\t"
      "v_pk_mul_f32 v[234:235], v[18:19], v[234:235]\n\t"
      "v_exp_f32 v232, v232\n\t"
      "v_exp_f32 v233, v233\n\t"
      "v_exp_f32 v234, v234\n\t"
      "v_exp_f32 v235, v235\n\t"
      "v_pk_mul_f32 v[16:17], v[16:17], v[44:45]\n\t"
      "v_pk_mul_f32 v[18:19], v[18:19], v[46:47]\n\t"
      "v_pk_add_f32 v[232:233], v[232:233], 1.0 op_sel_hi:[1,0]\n\t"
      "v_pk_add_f32 v[234:235], v[234:235], 1.0 op_sel_hi:[1,0]\n\t"
      "v_rcp_f32 v232, v232\n\t"
      "v_rcp_f32 v233, v233\n\t"
      "v_rcp_f32 v234, v234\n\t"
      "v_rcp_f32 v235, v235\n\t"
      "s_nop 0\n\t"
      "v_pk_mul_f32 v[16:17], v[16:17], v[232:233]\n\t"
      "v_pk_mul_f32 v[18:19], v[18:19], v[234:235]\n\t"
      "v_cvt_pk_bf16_f32 v240, v16, v17\n\t"
      "v_cvt_pk_bf16_f32 v241, v18, v19\n\t"
      "v_permlane32_swap_b32 v20, v48\n\t"
      "v_permlane32_swap_b32 v21, v49\n\t"
      "v_permlane32_swap_b32 v22, v50\n\t"
      "v_permlane32_swap_b32 v23, v51\n\t"
      "s_nop 0\n\t"
      "v_pk_mul_f32 v[236:237], v[20:21], v[20:21]\n\t"
      "v_pk_mul_f32 v[238:239], v[22:23], v[22:23]\n\t"
      "v_pk_fma_f32 v[236:237], v[236:237], s[54:55], v[248:249]\n\t"
      "v_pk_fma_f32 v[238:239], v[238:239], s[54:55], v[248:249]\n\t"
      "v_pk_mul_f32 v[236:237], v[20:21], v[236:237]\n\t"
      "v_pk_mul_f32 v[238:239], v[22:23], v[238:239]\n\t"
      "v_exp_f32 v236, v236\n\t"
      "v_exp_f32 v237, v237\n\t"
      "v_exp_f32 v238, v238\n\t"
      "v_exp_f32 v239, v239\n\t"
      "v_pk_mul_f32 v[20:21], v[20:21], v[48:49]\n\t"
      "v_pk_mul_f32 v[22:23], v[22:23], v[50:51]\n\t"
      "v_pk_add_f32 v[236:237], v[236:237], 1.0 op_sel_hi:[1,0]\n\t"
      "v_pk_add_f32 v[238:239], v[238:239], 1.0 op_sel_hi:[1,0]\n\t"
      "v_rcp_f32 v236, v236\n\t"
      "v_rcp_f32 v237, v237\n\t"
      "v_rcp_f32 v238, v238\n\t"
      "v_rcp_f32 v239, v239\n\t"
      "s_nop 0\n\t"
      "v_pk_mul_f32 v[20:21], v[20:21], v[236:237]\n\t"
      "v_pk_mul_f32 v[22:23], v[22:23], v[238:239]\n\t"
      "v_cvt_pk_bf16_f32 v242, v20, v21\n\t"
      "v_cvt_pk_bf16_f32 v243, v22, v23\n\t"
      "v_cndmask_b32 v244, v240, v242, s[58:59]\n\t"
      "v_cndmask_b32 v245, v241, v243, s[58:59]\n\t"
      "v_cndmask_b32 v246, v242, v240, s[58:59]\n\t"
      "v_cndmask_b32 v247, v243, v241, s[58:59]\n\t"
      "buffer_store_dwordx4 v[244:247], v230, %[srdH], s64 offen offset:1024 sc1\n\t"
      "v_permlane32_swap_b32 v24, v52\n\t"
      "v_permlane32_swap_b32 v25, v53\n\t"
      "v_permlane32_swap_b32 v26, v54\n\t"
      "v_permlane32_swap_b32 v27, v55\n\t"
      "s_nop 0\n\t"
      "v_pk_mul_f32 v[232:233], v[24:25], v[24:25]\n\t"
      "v_pk_mul_f32 v[234:235], v[26:27], v[26:27]\n\t"
      "v_pk_fma_f32 v[232:233], v[232:233], s[54:55], v[248:249]\n\t"
      "v_pk_fma_f32 v[234:235], v[234:235], s[54:55], v[248:249]\n\t"
      "v_pk_mul_f32 v[232:233], v[24:25], v[232:233]\n\t"
      "v_pk_mul_f32 v[234:235], v[26:27], v[234:235]\n\t"
      "v_exp_f32 v232, v232\n\t"
      "v_exp_f32 v233, v233\n\t"
      "v_exp_f32 v234, v234\n\t"
      "v_exp_f32 v235, v235\n\t"
      "v_pk_mul_f32 v[24:25], v[24:25], v[52:53]\n\t"
      "v_pk_mul_f32 v[26:27], v[26:27], v[54:55]\n\t"
      "v_pk_add_f32 v[232:233], v[232:233], 1.0 op_sel_hi:[1,0]\n\t"
      "v_pk_add_f32 v[234:235], v[234:235], 1.0 op_sel_hi:[1,0]\n\t"
      "v_rcp_f32 v232, v232\n\t"
      "v_rcp_f32 v233, v233\n\t"
      "v_rcp_f32 v234, v234\n\t"
      "v_rcp_f32 v235, v235\n\t"
      "s_nop 0\n\t"
      "v_pk_mul_f32 v[24:25], v[24:25], v[232:233]\n\t"
      "v_pk_mul_f32 v[26:27], v[26:27], v[234:235]\n\t"
      "v_cvt_pk_bf16_f32 v240, v24, v25\n\t"
      "v_cvt_pk_bf16_f32 v241, v26, v27\n\t"
      "buffer_store_dwordx2 v[240:241], v231, %[srdH], s64 offen offset:1536 sc1\n\t"
      "s_nop 1\n\t"
      "v_permlane32_swap_b32 v56, v84\n\t"
      "v_permlane32_swap_b32 v57, v85\n\t"
      "v_permlane32_swap_b32 v58, v86\n\t"
      "v_permlane32_swap_b32 v59, v87\n\t"
      "s_nop 0\n\t"
      "v_pk_mul_f32 v[232:233], v[56:57], v[56:57]\n\t"
      "v_pk_mul_f32 v[234:235], v[58:59], v[58:59]\n\t"
      "v_pk_fma_f32 v[232:233], v[232:233], s[54:55], v[248:249]\n\t"
      "v_pk_fma_f32 v[234:235], v[234:235], s[54:55], v[248:249]\n\t"
      "v_pk_mul_f32 v[232:233], v[56:57], v[232:233]\n\t"
      "v_pk_mul_f32 v[234:235], v[58:59], v[234:235]\n\t"
      "v_exp_f32 v232, v232\n\t"
      "v_exp_f32 v233, v233\n\t"
      "v_exp_f32 v234, v234\n\t"
      "v_exp_f32 v235, v235\n\t"
      "v_pk_mul_f32 v[56:57], v[56:57], v[84:85]\n\t"
      "v_pk_mul_f32 v[58:59], v[58:59], v[86:87]\n\t"
      "v_pk_add_f32 v[232:233], v[232:233], 1.0 op_sel_hi:[1,0]\n\t"
      "v_pk_add_f32 v[234:235], v[234:235], 1.0 op_sel_hi:[1,0]\n\t"
      "v_rcp_f32 v232, v232\n\t"
      "v_rcp_f32 v233, v233\n\t"
      "v_rcp_f32 v234, v234\n\t"
      "v_rcp_f32 v235, v235\n\t"
      "s_nop 0\n\t"
      "v_pk_mul_f32 v[56:57], v[56:57], v[232:233]\n\t"
      "v_pk_mul_f32 v[58:59], v[58:59], v[234:235]\n\t"
      "v_cvt_pk_bf16_f32 v240, v56, v57\n\t"
      "v_cvt_pk_bf16_f32 v241, v58, v59\n\t"
      "v_permlane32_swap_b32 v60, v88\n\t"
      "v_permlane32_swap_b32 v61, v89\n\t"
      "v_permlane32_swap_b32 v62, v90\n\t"
      "v_permlane32_swap_b32 v63, v91\n\t"
      "s_nop 0\n\t"
      "v_pk_mul_f32 v[236:237], v[60:61], v[60:61]\n\t"
      "v_pk_mul_f32 v[238:239], v[62:63], v[62:63]\n\t"
      "v_pk_fma_f32 v[236:237], v[236:237], s[54:55], v[248:249]\n\t"
      "v_pk_fma_f32 v[238:239], v[238:239], s[54:55], v[248:249]\n\t"
      "v_pk_mul_f32 v[236:237], v[60:61], v[236:237]\n\t"
      "v_pk_mul_f32 v[238:239], v[62:63], v[238:239]\n\t"
      "v_exp_f32 v236, v236\n\t"
      "v_exp_f32 v237, v237\n\t"
      "v_exp_f32 v238, v238\n\t"
      "v_exp_f32 v239, v239\n\t"
      "v_pk_mul_f32 v[60:61], v[60:61], v[88:89]\n\t"
      "v_pk_mul_f32 v[62:63], v[62:63], v[90:91]\n\t"
      "v_pk_add_f32 v[236:237], v[236:237], 1.0 op_sel_hi:[1,0]\n\t"
      "v_pk_add_f32 v[238:239], v[238:239], 1.0 op_sel_hi:[1,0]\n\t"
      "v_rcp_f32 v236, v236\n\t"
      "v_rcp_f32 v237, v237\n\t"
      "v_rcp_f32 v238, v238\n\t"
      "v_rcp_f32 v239, v239\n\t"
      "s_nop 0\n\t"
      "v_pk_mul_f32 v[60:61], v[60:61], v[236:237]\n\t"
      "v_pk_mul_f32 v[62:63], v[62:63], v[238:239]\n\t"
      "v_cvt_pk_bf16_f32 v242, v60, v61\n\t"
      "v_cvt_pk_bf16_f32 v243, v62, v63\n\t"
      "v_cndmask_b32 v244, v240, v242, s[58:59]\n\t"
      "v_cndmask_b32 v245, v241, v243, s[58:59]\n\t"
      "v_cndmask_b32 v246, v242, v240, s[58:59]\n\t"
      "v_cndmask_b32 v247, v243, v241, s[58:59]\n\t"
      "buffer_store_dwordx4 v[244:247], v230, %[srdH], s65 offen offset:0 sc1\n\t"
      "v_permlane32_swap_b32 v64, v92\n\t"
      "v_permlane32_swap_b32 v65, v93\n\t"
      "v_permlane32_swap_b32 v66, v94\n\t"
      "v_permlane32_swap_b32 v67, v95\n\t"
      "s_nop 0\n\t"
      "v_pk_mul_f32 v[232:233], v[64:65], v[64:65]\n\t"
      "v_pk_mul_f32 v[234:235], v[66:67], v[66:67]\n\t"
      "v_pk_fma_f32 v[232:233], v[232:233], s[54:55], v[248:249]\n\t"
      "v_pk_fma_f32 v[234:235], v[234:235], s[54:55], v[248:249]\n\t"
      "v_pk_mul_f32 v[232:233], v[64:65], v[232:233]\n\t"
      "v_pk_mul_f32 v[234:235], v[66:67], v[234:235]\n\t"
      "v_exp_f32 v232, v232\n\t"
      "v_exp_f32 v233, v233\n\t"
      "v_exp_f32 v234, v234\n\t"
      "v_exp_f32 v235, v235\n\t"
      "v_pk_mul_f32 v[64:65], v[64:65], v[92:93]\n\t"
      "v_pk_mul_f32 v[66:67], v[66:67], v[94:95]\n\t"
      "v_pk_add_f32 v[232:233], v[232:233], 1.0 op_sel_hi:[1,0]\n\t"
      "v_pk_add_f32 v[234:235], v[234:235], 1.0 op_sel_hi:[1,0]\n\t"
      "v_rcp_f32 v232, v232\n\t"
      "v_rcp_f32 v233, v233\n\t"
      "v_rcp_f32 v234, v234\n\t"
      "v_rcp_f32 v235, v235\n\t"
      "s_nop 0\n\t"
      "v_pk_mul_f32 v[64:65], v[64:65], v[232:233]\n\t"
      "v_pk_mul_f32 v[66:67], v[66:67], v[234:235]\n\t"
      "v_cvt_pk_bf16_f32 v240, v64, v65\n\t"
      "v_cvt_pk_bf16_f32 v241, v66, v67\n\t"
      "v_permlane32_swap_b32 v68, v96\n\t"
      "v_permlane32_swap_b32 v69, v97\n\t"
      "v_permlane32_swap_b32 v70, v98\n\t"
      "v_permlane32_swap_b32 v71, v99\n\t"
      "s_nop 0\n\t"
      "v_pk_mul_f32 v[236:237], v[68:69], v[68:69]\n\t"
      "v_pk_mul_f32 v[238:239], v[70:71], v[70:71]\n\t"
      "v_pk_fma_f32 v[236:237], v[236:237], s[54:55], v[248:249]\n\t"
      "v_pk_fma_f32 v[238:239], v[238:239], s[54:55], v[248:249]\n\t"
      "v_pk_mul_f32 v[236:237], v[68:69], v[236:237]\n\t"
      "v_pk_mul_f32 v[238:239], v[70:71], v[238:239]\n\t"
      "v_exp_f32 v236, v236\n\t"
      "v_exp_f32 v237, v237\n\t"
      "v_exp_f32 v238, v238\n\t"
      "v_exp_f32 v239, v239\n\t"
      "v_pk_mul_f32 v[68:69], v[68:69], v[96:97]\n\t"
      "v_pk_mul_f32 v[70:71], v[70:71], v[98:99]\n\t"
      "v_pk_add_f32 v[236:237], v[236:237], 1.0 op_sel_hi:[1,0]\n\t"
      "v_pk_add_f32 v[238:239], v[238:239], 1.0 op_sel_hi:[1,0]\n\t"
      "v_rcp_f32 v236, v236\n\t"
      "v_rcp_f32 v237, v237\n\t"
      "v_rcp_f32 v238, v238\n\t"
      "v_rcp_f32 v239, v239\n\t"
      "s_nop 0\n\t"
      "v_pk_mul_f32 v[68:69], v[68:69], v[236:237]\n\t"
      "v_pk_mul_f32 v[70:71], v[70:71], v[238:239]\n\t"
      "v_cvt_pk_bf16_f32 v242, v68, v69\n\t"
      "v_cvt_pk_bf16_f32 v243, v70, v71\n\t"
      "v_cndmask_b32 v244, v240, v242, s[58:59]\n\t"
      "v_cndmask_b32 v245, v241, v243, s[58:59]\n\t"
      "v_cndmask_b32 v246, v242, v240, s[58:59]\n\t"
      "v_cndmask_b32 v247, v243, v241, s[58:59]\n\t"
      "buffer_store_dwordx4 v[244:247], v230, %[srdH], s65 offen offset:512 sc1\n\t"
      "v_permlane32_swap_b32 v72, v100\n\t"
      "v_permlane32_swap_b32 v73, v101\n\t"
      "v_permlane32_swap_b32 v74, v102\n\t"
      "v_permlane32_swap_b32 v75, v103\n\t"
      "s_nop 0\n\t"
      "v_pk_mul_f32 v[232:233], v[72:73], v[72:73]\n\t"
      "v_pk_mul_f32 v[234:235], v[74:75], v[74:75]\n\t"
      "v_pk_fma_f32 v[232:233], v[232:233], s[54:55], v[248:249]\n\t"
      "v_pk_fma_f32 v[234:235], v[234:235], s[54:55], v[248:249]\n\t"
      "v_pk_mul_f32 v[232:233], v[72:73], v[232:233]\n\t"
      "v_pk_mul_f32 v[234:235], v[74:75], v[234:235]\n\t"
      "v_exp_f32 v232, v232\n\t"
      "v_exp_f32 v233, v233\n\t"
      "v_exp_f32 v234, v234\n\t"
      "v_exp_f32 v235, v235\n\t"
      "v_pk_mul_f32 v[72:73], v[72:73], v[100:101]\n\t"
      "v_pk_mul_f32 v[74:75], v[74:75], v[102:103]\n\t"
      "v_pk_add_f32 v[232:233], v[232:233], 1.0 op_sel_hi:[1,0]\n\t"
      "v_pk_add_f32 v[234:235], v[234:235], 1.0 op_sel_hi:[1,0]\n\t"
      "v_rcp_f32 v232, v232\n\t"
      "v_rcp_f32 v233, v233\n\t"
      "v_rcp_f32 v234, v234\n\t"
      "v_rcp_f32 v235, v235\n\t"
      "s_nop 0\n\t"
      "v_pk_mul_f32 v[72:73], v[72:73], v[232:233]\n\t"
      "v_pk_mul_f32 v[74:75], v[74:75], v[234:235]\n\t"
      "v_cvt_pk_bf16_f32 v240, v72, v73\n\t"
      "v_cvt_pk_bf16_f32 v241, v74, v75\n\t"
      "v_permlane32_swap_b32 v76, v104\n\t"
      "v_permlane32_swap_b32 v77, v105\n\t"
      "v_permlane32_swap_b32 v78, v106\n\t"
      "v_permlane32_swap_b32 v79, v107\n\t"
      "s_nop 0\n\t"
      "v_pk_mul_f32 v[236:237], v[76:77], v[76:77]\n\t"
      "v_pk_mul_f32 v[238:239], v[78:79], v[78:79]\n\t"
      "v_pk_fma_f32 v[236:237], v[236:237], s[54:55], v[248:249]\n\t"
      "v_pk_fma_f32 v[238:239], v[238:239], s[54:55], v[248:249]\n\t"
      "v_pk_mul_f32 v[236:237], v[76:77], v[236:237]\n\t"
      "v_pk_mul_f32 v[238:239], v[78:79], v[238:239]\n\t"
      "v_exp_f32 v236, v236\n\t"
      "v_exp_f32 v237, v237\n\t"
      "v_exp_f32 v238, v238\n\t"
      "v_exp_f32 v239, v239\n\t"
      "v_pk_mul_f32 v[76:77], v[76:77], v[104:105]\n\t"
      "v_pk_mul_f32 v[78:79], v[78:79], v[106:107]\n\t"
      "v_pk_add_f32 v[236:237], v[236:237], 1.0 op_sel_hi:[1,0]\n\t"
      "v_pk_add_f32 v[238:239], v[238:239], 1.0 op_sel_hi:[1,0]\n\t"
      "v_rcp_f32 v236, v236\n\t"
      "v_rcp_f32 v237, v237\n\t"
      "v_rcp_f32 v238, v238\n\t"
      "v_rcp_f32 v239, v239\n\t"
      "s_nop 0\n\t"
      "v_pk_mul_f32 v[76:77], v[76:77], v[236:237]\n\t"
      "v_pk_mul_f32 v[78:79], v[78:79], v[238:239]\n\t"
      "v_cvt_pk_bf16_f32 v242, v76, v77\n\t"
      "v_cvt_pk_bf16_f32 v243, v78, v79\n\t"
      "v_cndmask_b32 v244, v240, v242, s[58:59]\n\t"
      "v_cndmask_b32 v245, v241, v243, s[58:59]\n\t"
      "v_cndmask_b32 v246, v242, v240, s[58:59]\n\t"
      "v_cndmask_b32 v247, v243, v241, s[58:59]\n\t"
      "buffer_store_dwordx4 v[244:247], v230, %[srdH], s65 offen offset:1024 sc1\n\t"
      "v_permlane32_swap_b32 v80, v108\n\t"
      "v_permlane32_swap_b32 v81, v109\n\t"
      "v_permlane32_swap_b32 v82, v110\n\t"
      "v_permlane32_swap_b32 v83, v111\n\t"
      "s_nop 0\n\t"
      "v_pk_mul_f32 v[232:233], v[80:81], v[80:81]\n\t"
      "v_pk_mul_f32 v[234:235], v[82:83], v[82:83]\n\t"
      "v_pk_fma_f32 v[232:233], v[232:233], s[54:55], v[248:249]\n\t"
      "v_pk_fma_f32 v[234:235], v[234:235], s[54:55], v[248:249]\n\t"
      "v_pk_mul_f32 v[232:233], v[80:81], v[232:233]\n\t"
      "v_pk_mul_f32 v[234:235], v[82:83], v[234:235]\n\t"
      "v_exp_f32 v232, v232\n\t"
      "v_exp_f32 v233, v233\n\t"
      "v_exp_f32 v234, v234\n\t"
      "v_exp_f32 v235, v235\n\t"
      "v_pk_mul_f32 v[80:81], v[80:81], v[108:109]\n\t"
      "v_pk_mul_f32 v[82:83], v[82:83], v[110:111]\n\t"
      "v_pk_add_f32 v[232:233], v[232:233], 1.0 op_sel_hi:[1,0]\n\t"
      "v_pk_add_f32 v[234:235], v[234:235], 1.0 op_sel_hi:[1,0]\n\t"
      "v_rcp_f32 v232, v232\n\t"
      "v_rcp_f32 v233, v233\n\t"
      "v_rcp_f32 v234, v234\n\t"
      "v_rcp_f32 v235, v235\n\t"
      "s_nop 0\n\t"
      "v_pk_mul_f32 v[80:81], v[80:81], v[232:233]\n\t"
      "v_pk_mul_f32 v[82:83], v[82:83], v[234:235]\n\t"
      "v_cvt_pk_bf16_f32 v240, v80, v81\n\t"
      "v_cvt_pk_bf16_f32 v241, v82, v83\n\t"
      "buffer_store_dwordx2 v[240:241], v231, %[srdH], s65 offen offset:1536 sc1\n\t"
      "s_nop 1\n\t"
      "v_permlane32_swap_b32 v112, v140\n\t"
      "v_permlane32_swap_b32 v113, v141\n\t"
      "v_permlane32_swap_b32 v114, v142\n\t"
      "v_permlane32_swap_b32 v115, v143\n\t"
      "s_nop 0\n\t"
      "v_pk_mul_f32 v[232:233], v[112:113], v[112:113]\n\t"
      "v_pk_mul_f32 v[234:235], v[114:115], v[114:115]\n\t"
      "v_pk_fma_f32 v[232:233], v[232:233], s[54:55], v[248:249]\n\t"
      "v_pk_fma_f32 v[234:235], v[234:235], s[54:55], v[248:249]\n\t"
      "v_pk_mul_f32 v[232:233], v[112:113], v[232:233]\n\t"
      "v_pk_mul_f32 v[234:235], v[114:115], v[234:235]\n\t"
      "v_exp_f32 v232, v232\n\t"
      "v_exp_f32 v233, v233\n\t"
      "v_exp_f32 v234, v234\n\t"
      "v_exp_f32 v235, v235\n\t"
      "v_pk_mul_f32 v[112:113], v[112:113], v[140:141]\n\t"
      "v_pk_mul_f32 v[114:115], v[114:115], v[142:143]\n\t"
      "v_pk_add_f32 v[232:233], v[232:233], 1.0 op_sel_hi:[1,0]\n\t"
      "v_pk_add_f32 v[234:235], v[234:235], 1.0 op_sel_hi:[1,0]\n\t"
      "v_rcp_f32 v232, v232\n\t"
      "v_rcp_f32 v233, v233\n\t"
      "v_rcp_f32 v234, v234\n\t"
      "v_rcp_f32 v235, v235\n\t"
      "s_nop 0\n\t"
      "v_pk_mul_f32 v[112:113], v[112:113], v[232:233]\n\t"
      "v_pk_mul_f32 v[114:115], v[114:115], v[234:235]\n\t"
      "v_cvt_pk_bf16_f32 v240, v112, v113\n\t"
      "v_cvt_pk_bf16_f32 v241, v114, v115\n\t"
      "v_permlane32_swap_b32 v116, v144\n\t"
      "v_permlane32_swap_b32 v117, v145\n\t"
      "v_permlane32_swap_b32 v118, v146\n\t"
      "v_permlane32_swap_b32 v119, v147\n\t"
      "s_nop 0\n\t"
      "v_pk_mul_f32 v[236:237], v[116:117], v[116:117]\n\t"
      "v_pk_mul_f32 v[238:239], v[118:119], v[118:119]\n\t"
      "v_pk_fma_f32 v[236:237], v[236:237], s[54:55], v[248:249]\n\t"
      "v_pk_fma_f32 v[238:239], v[238:239], s[54:55], v[248:249]\n\t"
      "v_pk_mul_f32 v[236:237], v[116:117], v[236:237]\n\t"
      "v_pk_mul_f32 v[238:239], v[118:119], v[238:239]\n\t"
      "v_exp_f32 v236, v236\n\t"
      "v_exp_f32 v237, v237\n\t"
      "v_exp_f32 v238, v238\n\t"
      "v_exp_f32 v239, v239\n\t"
      "v_pk_mul_f32 v[116:117], v[116:117], v[144:145]\n\t"
      "v_pk_mul_f32 v[118:119], v[118:119], v[146:147]\n\t"
      "v_pk_add_f32 v[236:237], v[236:237], 1.0 op_sel_hi:[1,0]\n\t"
      "v_pk_add_f32 v[238:239], v[238:239], 1.0 op_sel_hi:[1,0]\n\t"
      "v_rcp_f32 v236, v236\n\t"
      "v_rcp_f32 v237, v237\n\t"
      "v_rcp_f32 v238, v238\n\t"
      "v_rcp_f32 v239, v239\n\t"
      "s_nop 0\n\t"
      "v_pk_mul_f32 v[116:117], v[116:117], v[236:237]\n\t"
      "v_pk_mul_f32 v[118:119], v[118:119], v[238:239]\n\t"
      "v_cvt_pk_bf16_f32 v242, v116, v117\n\t"
      "v_cvt_pk_bf16_f32 v243, v118, v119\n\t"
      "v_cndmask_b32 v244, v240, v242, s[58:59]\n\t"
      "v_cndmask_b32 v245, v241, v243, s[58:59]\n\t"
      "v_cndmask_b32 v246, v242, v240, s[58:59]\n\t"
      "v_cndmask_b32 v247, v243, v241, s[58:59]\n\t"
      "buffer_store_dwordx4 v[244:247], v230, %[srdH], s66 offen offset:0 sc1\n\t"
      "v_permlane32_swap_b32 v120, v148\n\t"
      "v_permlane32_swap_b32 v121, v149\n\t"
      "v_permlane32_swap_b32 v122, v150\n\t"
      "v_permlane32_swap_b32 v123, v151\n\t"
      "s_nop 0\n\t"
      "v_pk_mul_f32 v[232:233], v[120:121], v[120:121]\n\t"
      "v_pk_mul_f32 v[234:235], v[122:123], v[122:123]\n\t"
      "v_pk_fma_f32 v[232:233], v[232:233], s[54:55], v[248:249]\n\t"
      "v_pk_fma_f32 v[234:235], v[234:235], s[54:55], v[248:249]\n\t"
      "v_pk_mul_f32 v[232:233], v[120:121], v[232:233]\n\t"
      "v_pk_mul_f32 v[234:235], v[122:123], v[234:235]\n\t"
      "v_exp_f32 v232, v232\n\t"
      "v_exp_f32 v233, v233\n\t"
      "v_exp_f32 v234, v234\n\t"
      "v_exp_f32 v235, v235\n\t"
      "v_pk_mul_f32 v[120:121], v[120:121], v[148:149]\n\t"
      "v_pk_mul_f32 v[122:123], v[122:123], v[150:151]\n\t"
      "v_pk_add_f32 v[232:233], v[232:233], 1.0 op_sel_hi:[1,0]\n\t"
      "v_pk_add_f32 v[234:235], v[234:235], 1.0 op_sel_hi:[1,0]\n\t"
      "v_rcp_f32 v232, v232\n\t"
      "v_rcp_f32 v233, v233\n\t"
      "v_rcp_f32 v234, v234\n\t"
      "v_rcp_f32 v235, v235\n\t"
      "s_nop 0\n\t"
      "v_pk_mul_f32 v[120:121], v[120:121], v[232:233]\n\t"
      "v_pk_mul_f32 v[122:123], v[122:123], v[234:235]\n\t"
      "v_cvt_pk_bf16_f32 v240, v120, v121\n\t"
      "v_cvt_pk_bf16_f32 v241, v122, v123\n\t"
      "v_permlane32_swap_b32 v124, v152\n\t"
      "v_permlane32_swap_b32 v125, v153\n\t"
      "v_permlane32_swap_b32 v126, v154\n\t"
      "v_permlane32_swap_b32 v127, v155\n\t"
      "s_nop 0\n\t"
      "v_pk_mul_f32 v[236:237], v[124:125], v[124:125]\n\t"
      "v_pk_mul_f32 v[238:239], v[126:127], v[126:127]\n\t"
      "v_pk_fma_f32 v[236:237], v[236:237], s[54:55], v[248:249]\n\t"
      "v_pk_fma_f32 v[238:239], v[238:239], s[54:55], v[248:249]\n\t"
      "v_pk_mul_f32 v[236:237], v[124:125], v[236:237]\n\t"
      "v_pk_mul_f32 v[238:239], v[126:127], v[238:239]\n\t"
      "v_exp_f32 v236, v236\n\t"
      "v_exp_f32 v237, v237\n\t"
      "v_exp_f32 v238, v238\n\t"
      "v_exp_f32 v239, v239\n\t"
      "v_pk_mul_f32 v[124:125], v[124:125], v[152:153]\n\t"
      "v_pk_mul_f32 v[126:127], v[126:127], v[154:155]\n\t"
      "v_pk_add_f32 v[236:237], v[236:237], 1.0 op_sel_hi:[1,0]\n\t"
      "v_pk_add_f32 v[238:239], v[238:239], 1.0 op_sel_hi:[1,0]\n\t"
      "v_rcp_f32 v236, v236\n\t"
      "v_rcp_f32 v237, v237\n\t"
      "v_rcp_f32 v238, v238\n\t"
      "v_rcp_f32 v239, v239\n\t"
      "s_nop 0\n\t"
      "v_pk_mul_f32 v[124:125], v[124:125], v[236:237]\n\t"
      "v_pk_mul_f32 v[126:127], v[126:127], v[238:239]\n\t"
      "v_cvt_pk_bf16_f32 v242, v124, v125\n\t"
      "v_cvt_pk_bf16_f32 v243, v126, v127\n\t"
      "v_cndmask_b32 v244, v240, v242, s[58:59]\n\t"
      "v_cndmask_b32 v245, v241, v243, s[58:59]\n\t"
      "v_cndmask_b32 v246, v242, v240, s[58:59]\n\t"
      "v_cndmask_b32 v247, v243, v241, s[58:59]\n\t"
      "buffer_store_dwordx4 v[244:247], v230, %[srdH], s66 offen offset:512 sc1\n\t"
      "v_permlane32_swap_b32 v128, v156\n\t"
      "v_permlane32_swap_b32 v129, v157\n\t"
      "v_permlane32_swap_b32 v130, v158\n\t"
      "v_permlane32_swap_b32 v131, v159\n\t"
      "s_nop 0\n\t"
      "v_pk_mul_f32 v[232:233], v[128:129], v[128:129]\n\t"
      "v_pk_mul_f32 v[234:235], v[130:131], v[130:131]\n\t"
      "v_pk_fma_f32 v[232:233], v[232:233], s[54:55], v[248:249]\n\t"
      "v_pk_fma_f32 v[234:235], v[234:235], s[54:55], v[248:249]\n\t"
      "v_pk_mul_f32 v[232:233], v[128:129], v[232:233]\n\t"
      "v_pk_mul_f32 v[234:235], v[130:131], v[234:235]\n\t"
      "v_exp_f32 v232, v232\n\t"
      "v_exp_f32 v233, v233\n\t"
      "v_exp_f32 v234, v234\n\t"
      "v_exp_f32 v235, v235\n\t"
      "v_pk_mul_f32 v[128:129], v[128:129], v[156:157]\n\t"
      "v_pk_mul_f32 v[130:131], v[130:131], v[158:159]\n\t"
      "v_pk_add_f32 v[232:233], v[232:233], 1.0 op_sel_hi:[1,0]\n\t"
      "v_pk_add_f32 v[234:235], v[234:235], 1.0 op_sel_hi:[1,0]\n\t"
      "v_rcp_f32 v232, v232\n\t"
      "v_rcp_f32 v233, v233\n\t"
      "v_rcp_f32 v234, v234\n\t"
      "v_rcp_f32 v235, v235\n\t"
      "s_nop 0\n\t"
      "v_pk_mul_f32 v[128:129], v[128:129], v[232:233]\n\t"
      "v_pk_mul_f32 v[130:131], v[130:131], v[234:235]\n\t"
      "v_cvt_pk_bf16_f32 v240, v128, v129\n\t"
      "v_cvt_pk_bf16_f32 v241, v130, v131\n\t"
      "v_permlane32_swap_b32 v132, v160\n\t"
      "v_permlane32_swap_b32 v133, v161\n\t"
      "v_permlane32_swap_b32 v134, v162\n\t"
      "v_permlane32_swap_b32 v135, v163\n\t"
      "s_nop 0\n\t"
      "v_pk_mul_f32 v[236:237], v[132:133], v[132:133]\n\t"
      "v_pk_mul_f32 v[238:239], v[134:135], v[134:135]\n\t"
      "v_pk_fma_f32 v[236:237], v[236:237], s[54:55], v[248:249]\n\t"
      "v_pk_fma_f32 v[238:239], v[238:239], s[54:55], v[248:249]\n\t"
      "v_pk_mul_f32 v[236:237], v[132:133], v[236:237]\n\t"
      "v_pk_mul_f32 v[238:239], v[134:135], v[238:239]\n\t"
      "v_exp_f32 v236, v236\n\t"
      "v_exp_f32 v237, v237\n\t"
      "v_exp_f32 v238, v238\n\t"
      "v_exp_f32 v239, v239\n\t"
      "v_pk_mul_f32 v[132:133], v[132:133], v[160:161]\n\t"
      "v_pk_mul_f32 v[134:135], v[134:135], v[162:163]\n\t"
      "v_pk_add_f32 v[236:237], v[236:237], 1.0 op_sel_hi:[1,0]\n\t"
      "v_pk_add_f32 v[238:239], v[238:239], 1.0 op_sel_hi:[1,0]\n\t"
      "v_rcp_f32 v236, v236\n\t"
      "v_rcp_f32 v237, v237\n\t"
      "v_rcp_f32 v238, v238\n\t"
      "v_rcp_f32 v239, v239\n\t"
      "s_nop 0\n\t"
      "v_pk_mul_f32 v[132:133], v[132:133], v[236:237]\n\t"
      "v_pk_mul_f32 v[134:135], v[134:135], v[238:239]\n\t"
      "v_cvt_pk_bf16_f32 v242, v132, v133\n\t"
      "v_cvt_pk_bf16_f32 v243, v134, v135\n\t"
      "v_cndmask_b32 v244, v240, v242, s[58:59]\n\t"
      "v_cndmask_b32 v245, v241, v243, s[58:59]\n\t"
      "v_cndmask_b32 v246, v242, v240, s[58:59]\n\t"
      "v_cndmask_b32 v247, v243, v241, s[58:59]\n\t"
      "buffer_store_dwordx4 v[244:247], v230, %[srdH], s66 offen offset:1024 sc1\n\t"
      "v_permlane32_swap_b32 v136, v164\n\t"
      "v_permlane32_swap_b32 v137, v165\n\t"
      "v_permlane32_swap_b32 v138, v166\n\t"
      "v_permlane32_swap_b32 v139, v167\n\t"
      "s_nop 0\n\t"
      "v_pk_mul_f32 v[232:233], v[136:137], v[136:137]\n\t"
      "v_pk_mul_f32 v[234:235], v[138:139], v[138:139]\n\t"
      "v_pk_fma_f32 v[232:233], v[232:233], s[54:55], v[248:249]\n\t"
      "v_pk_fma_f32 v[234:235], v[234:235], s[54:55], v[248:249]\n\t"
      "v_pk_mul_f32 v[232:233], v[136:137], v[232:233]\n\t"
      "v_pk_mul_f32 v[234:235], v[138:139], v[234:235]\n\t"
      "v_exp_f32 v232, v232\n\t"
      "v_exp_f32 v233, v233\n\t"
      "v_exp_f32 v234, v234\n\t"
      "v_exp_f32 v235, v235\n\t"
      "v_pk_mul_f32 v[136:137], v[136:137], v[164:165]\n\t"
      "v_pk_mul_f32 v[138:139], v[138:139], v[166:167]\n\t"
      "v_pk_add_f32 v[232:233], v[232:233], 1.0 op_sel_hi:[1,0]\n\t"
      "v_pk_add_f32 v[234:235], v[234:235], 1.0 op_sel_hi:[1,0]\n\t"
      "v_rcp_f32 v232, v232\n\t"
      "v_rcp_f32 v233, v233\n\t"
      "v_rcp_f32 v234, v234\n\t"
      "v_rcp_f32 v235, v235\n\t"
      "s_nop 0\n\t"
      "v_pk_mul_f32 v[136:137], v[136:137], v[232:233]\n\t"
      "v_pk_mul_f32 v[138:139], v[138:139], v[234:235]\n\t"
      "v_cvt_pk_bf16_f32 v240, v136, v137\n\t"
      "v_cvt_pk_bf16_f32 v241, v138, v139\n\t"
      "buffer_store_dwordx2 v[240:241], v231, %[srdH], s66 offen offset:1536 sc1\n\t"
      "s_nop 1\n\t"
      "v_permlane32_swap_b32 v168, v196\n\t"
      "v_permlane32_swap_b32 v169, v197\n\t"
      "v_permlane32_swap_b32 v170, v198\n\t"
      "v_permlane32_swap_b32 v171, v199\n\t"
      "s_nop 0\n\t"
      "v_pk_mul_f32 v[232:233], v[168:169], v[168:169]\n\t"
      "v_pk_mul_f32 v[234:235], v[170:171], v[170:171]\n\t"
      "v_pk_fma_f32 v[232:233], v[232:233], s[54:55], v[248:249]\n\t"
      "v_pk_fma_f32 v[234:235], v[234:235], s[54:55], v[248:249]\n\t"
      "v_pk_mul_f32 v[232:233], v[168:169], v[232:233]\n\t"
      "v_pk_mul_f32 v[234:235], v[170:171], v[234:235]\n\t"
      "v_exp_f32 v232, v232\n\t"
      "v_exp_f32 v233, v233\n\t"
      "v_exp_f32 v234, v234\n\t"
      "v_exp_f32 v235, v235\n\t"
      "v_pk_mul_f32 v[168:169], v[168:169], v[196:197]\n\t"
      "v_pk_mul_f32 v[170:171], v[170:171], v[198:199]\n\t"
      "v_pk_add_f32 v[232:233], v[232:233], 1.0 op_sel_hi:[1,0]\n\t"
      "v_pk_add_f32 v[234:235], v[234:235], 1.0 op_sel_hi:[1,0]\n\t"
      "v_rcp_f32 v232, v232\n\t"
      "v_rcp_f32 v233, v233\n\t"
      "v_rcp_f32 v234, v234\n\t"
      "v_rcp_f32 v235, v235\n\t"
      "s_nop 0\n\t"
      "v_pk_mul_f32 v[168:169], v[168:169], v[232:233]\n\t"
      "v_pk_mul_f32 v[170:171], v[170:171], v[234:235]\n\t"
      "v_cvt_pk_bf16_f32 v240, v168, v169\n\t"
      "v_cvt_pk_bf16_f32 v241, v170, v171\n\t"
      "v_permlane32_swap_b32 v172, v200\n\t"
      "v_permlane32_swap_b32 v173, v201\n\t"
      "v_permlane32_swap_b32 v174, v202\n\t"
      "v_permlane32_swap_b32 v175, v203\n\t"
      "s_nop 0\n\t"
      "v_pk_mul_f32 v[236:237], v[172:173], v[172:173]\n\t"
      "v_pk_mul_f32 v[238:239], v[174:175], v[174:175]\n\t"
      "v_pk_fma_f32 v[236:237], v[236:237], s[54:55], v[248:249]\n\t"
      "v_pk_fma_f32 v[238:239], v[238:239], s[54:55], v[248:249]\n\t"
      "v_pk_mul_f32 v[236:237], v[172:173], v[236:237]\n\t"
      "v_pk_mul_f32 v[238:239], v[174:175], v[238:239]\n\t"
      "v_exp_f32 v236, v236\n\t"
      "v_exp_f32 v237, v237\n\t"
      "v_exp_f32 v238, v238\n\t"
      "v_exp_f32 v239, v239\n\t"
      "v_pk_mul_f32 v[172:173], v[172:173], v[200:201]\n\t"
      "v_pk_mul_f32 v[174:175], v[174:175], v[202:203]\n\t"
      "v_pk_add_f32 v[236:237], v[236:237], 1.0 op_sel_hi:[1,0]\n\t"
      "v_pk_add_f32 v[238:239], v[238:239], 1.0 op_sel_hi:[1,0]\n\t"
      "v_rcp_f32 v236, v236\n\t"
      "v_rcp_f32 v237, v237\n\t"
      "v_rcp_f32 v238, v238\n\t"
      "v_rcp_f32 v239, v239\n\t"
      "s_nop 0\n\t"
      "v_pk_mul_f32 v[172:173], v[172:173], v[236:237]\n\t"
      "v_pk_mul_f32 v[174:175], v[174:175], v[238:239]\n\t"
      "v_cvt_pk_bf16_f32 v242, v172, v173\n\t"
      "v_cvt_pk_bf16_f32 v243, v174, v175\n\t"
      "v_cndmask_b32 v244, v240, v242, s[58:59]\n\t"
      "v_cndmask_b32 v245, v241, v243, s[58:59]\n\t"
      "v_cndmask_b32 v246, v242, v240, s[58:59]\n\t"
      "v_cndmask_b32 v247, v243, v241, s[58:59]\n\t"
      "buffer_store_dwordx4 v[244:247], v230, %[srdH], s67 offen offset:0 sc1\n\t"
      "v_permlane32_swap_b32 v176, v204\n\t"
      "v_permlane32_swap_b32 v177, v205\n\t"
      "v_permlane32_swap_b32 v178, v206\n\t"
      "v_permlane32_swap_b32 v179, v207\n\t"
      "s_nop 0\n\t"
      "v_pk_mul_f32 v[232:233], v[176:177], v[176:177]\n\t"
      "v_pk_mul_f32 v[234:235], v[178:179], v[178:179]\n\t"
      "v_pk_fma_f32 v[232:233], v[232:233], s[54:55], v[248:249]\n\t"
      "v_pk_fma_f32 v[234:235], v[234:235], s[54:55], v[248:249]\n\t"
      "v_pk_mul_f32 v[232:233], v[176:177], v[232:233]\n\t"
      "v_pk_mul_f32 v[234:235], v[178:179], v[234:235]\n\t"
      "v_exp_f32 v232, v232\n\t"
      "v_exp_f32 v233, v233\n\t"
      "v_exp_f32 v234, v234\n\t"
      "v_exp_f32 v235, v235\n\t"
      "v_pk_mul_f32 v[176:177], v[176:177], v[204:205]\n\t"
      "v_pk_mul_f32 v[178:179], v[178:179], v[206:207]\n\t"
      "v_pk_add_f32 v[232:233], v[232:233], 1.0 op_sel_hi:[1,0]\n\t"
      "v_pk_add_f32 v[234:235], v[234:235], 1.0 op_sel_hi:[1,0]\n\t"
      "v_rcp_f32 v232, v232\n\t"
      "v_rcp_f32 v233, v233\n\t"
      "v_rcp_f32 v234, v234\n\t"
      "v_rcp_f32 v235, v235\n\t"
      "s_nop 0\n\t"
      "v_pk_mul_f32 v[176:177], v[176:177], v[232:233]\n\t"
      "v_pk_mul_f32 v[178:179], v[178:179], v[234:235]\n\t"
      "v_cvt_pk_bf16_f32 v240, v176, v177\n\t"
      "v_cvt_pk_bf16_f32 v241, v178, v179\n\t"
      "v_permlane32_swap_b32 v180, v208\n\t"
      "v_permlane32_swap_b32 v181, v209\n\t"
      "v_permlane32_swap_b32 v182, v210\n\t"
      "v_permlane32_swap_b32 v183, v211\n\t"
      "s_nop 0\n\t"
      "v_pk_mul_f32 v[236:237], v[180:181], v[180:181]\n\t"
      "v_pk_mul_f32 v[238:239], v[182:183], v[182:183]\n\t"
      "v_pk_fma_f32 v[236:237], v[236:237], s[54:55], v[248:249]\n\t"
      "v_pk_fma_f32 v[238:239], v[238:239], s[54:55], v[248:249]\n\t"
      "v_pk_mul_f32 v[236:237], v[180:181], v[236:237]\n\t"
      "v_pk_mul_f32 v[238:239], v[182:183], v[238:239]\n\t"
      "v_exp_f32 v236, v236\n\t"
      "v_exp_f32 v237, v237\n\t"
      "v_exp_f32 v238, v238\n\t"
      "v_exp_f32 v239, v239\n\t"
      "v_pk_mul_f32 v[180:181], v[180:181], v[208:209]\n\t"
      "v_pk_mul_f32 v[182:183], v[182:183], v[210:211]\n\t"
      "v_pk_add_f32 v[236:237], v[236:237], 1.0 op_sel_hi:[1,0]\n\t"
      "v_pk_add_f32 v[238:239], v[238:239], 1.0 op_sel_hi:[1,0]\n\t"
      "v_rcp_f32 v236, v236\n\t"
      "v_rcp_f32 v237, v237\n\t"
      "v_rcp_f32 v238, v238\n\t"
      "v_rcp_f32 v239, v239\n\t"
      "s_nop 0\n\t"
      "v_pk_mul_f32 v[180:181], v[180:181], v[236:237]\n\t"
      "v_pk_mul_f32 v[182:183], v[182:183], v[238:239]\n\t"
      "v_cvt_pk_bf16_f32 v242, v180, v181\n\t"
      "v_cvt_pk_bf16_f32 v243, v182, v183\n\t"
      "v_cndmask_b32 v244, v240, v242, s[58:59]\n\t"
      "v_cndmask_b32 v245, v241, v243, s[58:59]\n\t"
      "v_cndmask_b32 v246, v242, v240, s[58:59]\n\t"
      "v_cndmask_b32 v247, v243, v241, s[58:59]\n\t"
      "buffer_store_dwordx4 v[244:247], v230, %[srdH], s67 offen offset:512 sc1\n\t"
      "v_permlane32_swap_b32 v184, v212\n\t"
      "v_permlane32_swap_b32 v185, v213\n\t"
      "v_permlane32_swap_b32 v186, v214\n\t"
      "v_permlane32_swap_b32 v187, v215\n\t"
      "s_nop 0\n\t"
      "v_pk_mul_f32 v[232:233], v[184:185], v[184:185]\n\t"
      "v_pk_mul_f32 v[234:235], v[186:187], v[186:187]\n\t"
      "v_pk_fma_f32 v[232:233], v[232:233], s[54:55], v[248:249]\n\t"
      "v_pk_fma_f32 v[234:235], v[234:235], s[54:55], v[248:249]\n\t"
      "v_pk_mul_f32 v[232:233], v[184:185], v[232:233]\n\t"
      "v_pk_mul_f32 v[234:235], v[186:187], v[234:235]\n\t"
      "v_exp_f32 v232, v232\n\t"
      "v_exp_f32 v233, v233\n\t"
      "v_exp_f32 v234, v234\n\t"
      "v_exp_f32 v235, v235\n\t"
      "v_pk_mul_f32 v[184:185], v[184:185], v[212:213]\n\t"
      "v_pk_mul_f32 v[186:187], v[186:187], v[214:215]\n\t"
      "v_pk_add_f32 v[232:233], v[232:233], 1.0 op_sel_hi:[1,0]\n\t"
      "v_pk_add_f32 v[234:235], v[234:235], 1.0 op_sel_hi:[1,0]\n\t"
      "v_rcp_f32 v232, v232\n\t"
      "v_rcp_f32 v233, v233\n\t"
      "v_rcp_f32 v234, v234\n\t"
      "v_rcp_f32 v235, v235\n\t"
      "s_nop 0\n\t"
      "v_pk_mul_f32 v[184:185], v[184:185], v[232:233]\n\t"
      "v_pk_mul_f32 v[186:187], v[186:187], v[234:235]\n\t"
      "v_cvt_pk_bf16_f32 v240, v184, v185\n\t"
      "v_cvt_pk_bf16_f32 v241, v186, v187\n\t"
      "v_permlane32_swap_b32 v188, v216\n\t"
      "v_permlane32_swap_b32 v189, v217\n\t"
      "v_permlane32_swap_b32 v190, v218\n\t"
      "v_permlane32_swap_b32 v191, v219\n\t"
      "s_nop 0\n\t"
      "v_pk_mul_f32 v[236:237], v[188:189], v[188:189]\n\t"
      "v_pk_mul_f32 v[238:239], v[190:191], v[190:191]\n\t"
      "v_pk_fma_f32 v[236:237], v[236:237], s[54:55], v[248:249]\n\t"
      "v_pk_fma_f32 v[238:239], v[238:239], s[54:55], v[248:249]\n\t"
      "v_pk_mul_f32 v[236:237], v[188:189], v[236:237]\n\t"
      "v_pk_mul_f32 v[238:239], v[190:191], v[238:239]\n\t"
      "v_exp_f32 v236, v236\n\t"
      "v_exp_f32 v237, v237\n\t"
      "v_exp_f32 v238, v238\n\t"
      "v_exp_f32 v239, v239\n\t"
      "v_pk_mul_f32 v[188:189], v[188:189], v[216:217]\n\t"
      "v_pk_mul_f32 v[190:191], v[190:191], v[218:219]\n\t"
      "v_pk_add_f32 v[236:237], v[236:237], 1.0 op_sel_hi:[1,0]\n\t"
      "v_pk_add_f32 v[238:239], v[238:239], 1.0 op_sel_hi:[1,0]\n\t"
      "v_rcp_f32 v236, v236\n\t"
      "v_rcp_f32 v237, v237\n\t"
      "v_rcp_f32 v238, v238\n\t"
      "v_rcp_f32 v239, v239\n\t"
      "s_nop 0\n\t"
      "v_pk_mul_f32 v[188:189], v[188:189], v[236:237]\n\t"
      "v_pk_mul_f32 v[190:191], v[190:191], v[238:239]\n\t"
      "v_cvt_pk_bf16_f32 v242, v188, v189\n\t"
      "v_cvt_pk_bf16_f32 v243, v190, v191\n\t"
      "v_cndmask_b32 v244, v240, v242, s[58:59]\n\t"
      "v_cndmask_b32 v245, v241, v243, s[58:59]\n\t"
      "v_cndmask_b32 v246, v242, v240, s[58:59]\n\t"
      "v_cndmask_b32 v247, v243, v241, s[58:59]\n\t"
      "buffer_store_dwordx4 v[244:247], v230, %[srdH], s67 offen offset:1024 sc1\n\t"
      "v_permlane32_swap_b32 v192, v220\n\t"
      "v_permlane32_swap_b32 v193, v221\n\t"
      "v_permlane32_swap_b32 v194, v222\n\t"
      "v_permlane32_swap_b32 v195, v223\n\t"
      "s_nop 0\n\t"
      "v_pk_mul_f32 v[232:233], v[192:193], v[192:193]\n\t"
      "v_pk_mul_f32 v[234:235], v[194:195], v[194:195]\n\t"
      "v_pk_fma_f32 v[232:233], v[232:233], s[54:55], v[248:249]\n\t"
      "v_pk_fma_f32 v[234:235], v[234:235], s[54:55], v[248:249]\n\t"
      "v_pk_mul_f32 v[232:233], v[192:193], v[232:233]\n\t"
      "v_pk_mul_f32 v[234:235], v[194:195], v[234:235]\n\t"
      "v_exp_f32 v232, v232\n\t"
      "v_exp_f32 v233, v233\n\t"
      "v_exp_f32 v234, v234\n\t"
      "v_exp_f32 v235, v235\n\t"
      "v_pk_mul_f32 v[192:193], v[192:193], v[220:221]\n\t"
      "v_pk_mul_f32 v[194:195], v[194:195], v[222:223]\n\t"
      "v_pk_add_f32 v[232:233], v[232:233], 1.0 op_sel_hi:[1,0]\n\t"
      "v_pk_add_f32 v[234:235], v[234:235], 1.0 op_sel_hi:[1,0]\n\t"
      "v_rcp_f32 v232, v232\n\t"
      "v_rcp_f32 v233, v233\n\t"
      "v_rcp_f32 v234, v234\n\t"
      "v_rcp_f32 v235, v235\n\t"
      "s_nop 0\n\t"
      "v_pk_mul_f32 v[192:193], v[192:193], v[232:233]\n\t"
      "v_pk_mul_f32 v[194:195], v[194:195], v[234:235]\n\t"
      "v_cvt_pk_bf16_f32 v240, v192, v193\n\t"
      "v_cvt_pk_bf16_f32 v241, v194, v195\n\t"
      "buffer_store_dwordx2 v[240:241], v231, %[srdH], s67 offen offset:1536 sc1\n\t"
      "s_nop 1\n\t"
      "s_nop 7\n\t"
      "s_nop 7\n\t"
      : "+{v[0:31]}"(A0), "+{v[32:63]}"(A1), "+{v[64:95]}"(A2), "+{v[96:127]}"(A3), "+{v[128:159]}"(A4), "+{v[160:191]}"(A5), "+{v[192:223]}"(A6), "+{v[224:231]}"(r0), "+{v[248:255]}"(tv), "+{v[232:239]}"(vd)
      : [srd] "s"(srd), [lds] "s"(ldsd), [wid] "s"(wid), [srdH] "s"(srdH), "{s[58:59]}"(oddm), [srdW2] "s"(srdW2), [ldsW2] "s"(ldsW2)
      : "memory", "scc", "s54", "s55", "s64", "s65", "s66", "s67", "s70", "v240", "v241", "v242", "v243", "v244", "v245", "v246", "v247", "a0", "a1", "a2", "a3", "a4", "a5", "a6", "a7", "a8", "a9", "a10", "a11", "a12", "a13", "a14", "a15", "a16", "a17", "a18", "a19", "a20", "a21", "a22", "a23", "a24", "a25", "a26", "a27", "a28", "a29", "a30", "a31", "a32", "a33", "a34", "a35", "a36", "a37", "a38", "a39", "a40", "a41", "a42", "a43", "a44", "a45", "a46", "a47", "a48", "a49", "a50", "a51", "a52", "a53", "a54", "a55", "a56", "a57", "a58", "a59", "a60", "a61", "a62", "a63", "a64", "a65", "a66", "a67", "a68", "a69", "a70", "a71", "a72", "a73", "a74", "a75", "a76", "a77", "a78", "a79", "a80", "a81", "a82", "a83", "a84", "a85", "a86", "a87", "a88", "a89", "a90", "a91", "a92", "a93", "a94", "a95", "a96", "a97", "a98", "a99", "a100", "a101", "a102", "a103", "a104", "a105", "a106", "a107", "a108", "a109", "a110", "a111", "a112", "a113", "a114", "a115", "a116", "a117", "a118", "a119", "s40", "s41", "s42", "s43", "s44", "s45", "s46", "s47", "s48", "s49", "s51", "s56", "vcc");
    DRAIN();
    BARRIER();
    if (tid == 0) signal_flag(flagC + pm * 32 + pn, tag);
  }

  {
    int ln = lane; asm volatile("" : "+v"(ln));
    const int wm = wid >> 1, wn = wid & 1, c = ln & 15, q = ln >> 4;
    const int kh = xcd & 1, pm2 = ((xcd >> 1) & 1) * 8 + (idx & 7), pn2 = (xcd >> 2) * 4 + (idx >> 3);
    const int tile = pm2 * 8 + pn2, t2 = pm2 * 128, d0 = pn2 * 128;
    f32x32 A0, A1;
    const int g = q, qr = (ln >> 2) & 3, p = ln & 3, fsw = qr | ((g & 1) << 2);
    u32x8 r0, vd;
    r0[0] = sbase + wm * 8192 + q * 256 + c * 16;
    const u32 wb = sbase + G2_WOFF + (8 * g + qr) * 256 + (p & 1) * 8;
#pragma unroll
    for (int nb = 0; nb < 4; ++nb) r0[1 + nb] = wb + (((wn * 8 + 2 * nb + (p >> 1)) ^ (fsw << 1)) * 16);
    r0[5] = r0[6] = r0[7] = 0;
    vd[0] = ln * 16 + (2 * wid) * 114688; vd[1] = ln * 16 + (2 * wid + 1) * 114688;
#pragma unroll
    for (int j = 0; j < 4; ++j) {
      const int i = 4 * wid + j, r4 = ln >> 4, f = r4 | (((i >> 1) & 1) << 2), chunk = (ln & 15) ^ (f << 1);
      vd[2 + j] = (4 * i + r4) * 2048 + chunk * 16 - j * 1024 + 4096;
    }
    vd[6] = vd[7] = 0;
    const auto srdA = mk_rsrc((const char*)Hb + (size_t)(t2 / 16) * 114688 + kh * 57344, 8 * 114688 - kh * 57344);
    const auto srdW = mk_rsrc((const char*)wd + (size_t)kh * 1792 * 2048 + d0 * 2 - 4096, (3584 - kh * 1792) * 2048 - d0 * 2 + 4096);
    const u32 ldsA = sbase + 2 * wid * 2048, ldsW = sbase + G2_WOFF + 4 * wid * 1024;
    if (wid == 0) poll_flags(flagC + (pm2 >> 1) * 32 + kh * 16, 16, tag, lane);
    BARRIER();
    asm volatile(
      "s_nop 4\n\t"
      "s_mov_b32 s40, 0\n\t"
      "s_mov_b32 s42, %[ldsA]\n\t"
      "s_add_u32 m0, s42, 0\n\t"
      "s_nop 0\n\t"
      "buffer_load_dwordx4 v72, %[srdA], s40 offen offset:0 sc1 lds\n\t"
      "buffer_load_dwordx4 v72, %[srdA], s40 offen offset:1024 sc1 lds\n\t"
      "s_add_u32 m0, s42, 2048\n\t"
      "s_nop 0\n\t"
      "buffer_load_dwordx4 v73, %[srdA], s40 offen offset:0 sc1 lds\n\t"
      "buffer_load_dwordx4 v73, %[srdA], s40 offen offset:1024 sc1 lds\n\t"
      "s_add_u32 s40, s40, 2048\n\t"
      "s_add_u32 s42, s42, 32768\n\t"
      "v_mov_b32 v0, 0\n\t"
      "v_mov_b32 v1, 0\n\t"
      "v_mov_b32 v2, 0\n\t"
      "v_mov_b32 v3, 0\n\t"
      "v_mov_b32 v4, 0\n\t"
      "v_mov_b32 v5, 0\n\t"
      "v_mov_b32 v6, 0\n\t"
      "v_mov_b32 v7, 0\n\t"
      "v_mov_b32 v8, 0\n\t"
      "v_mov_b32 v9, 0\n\t"
      "v_mov_b32 v10, 0\n\t"
      "v_mov_b32 v11, 0\n\t"
      "v_mov_b32 v12, 0\n\t"
      "v_mov_b32 v13, 0\n\t"
      "v_mov_b32 v14, 0\n\t"
      "v_mov_b32 v15, 0\n\t"
      "v_mov_b32 v16, 0\n\t"
      "v_mov_b32 v17, 0\n\t"
      "v_mov_b32 v18, 0\n\t"
      "v_mov_b32 v19, 0\n\t"
      "v_mov_b32 v20, 0\n\t"
      "v_mov_b32 v21, 0\n\t"
      "v_mov_b32 v22, 0\n\t"
      "v_mov_b32 v23, 0\n\t"
      "v_mov_b32 v24, 0\n\t"
      "v_mov_b32 v25, 0\n\t"
      "v_mov_b32 v26, 0\n\t"
      "v_mov_b32 v27, 0\n\t"
      "v_mov_b32 v28, 0\n\t"
      "v_mov_b32 v29, 0\n\t"
      "v_mov_b32 v30, 0\n\t"
      "v_mov_b32 v31, 0\n\t"
      "v_mov_b32 v32, 0\n\t"
      "v_mov_b32 v33, 0\n\t"
      "v_mov_b32 v34, 0\n\t"
      "v_mov_b32 v35, 0\n\t"
      "v_mov_b32 v36, 0\n\t"
      "v_mov_b32 v37, 0\n\t"
      "v_mov_b32 v38, 0\n\t"
      "v_mov_b32 v39, 0\n\t"
      "v_mov_b32 v40, 0\n\t"
      "v_mov_b32 v41, 0\n\t"
      "v_mov_b32 v42, 0\n\t"
      "v_mov_b32 v43, 0\n\t"
      "v_mov_b32 v44, 0\n\t"
      "v_mov_b32 v45, 0\n\t"
      "v_mov_b32 v46, 0\n\t"
      "v_mov_b32 v47, 0\n\t"
      "v_mov_b32 v48, 0\n\t"
      "v_mov_b32 v49, 0\n\t"
      "v_mov_b32 v50, 0\n\t"
      "v_mov_b32 v51, 0\n\t"
      "v_mov_b32 v52, 0\n\t"
      "v_mov_b32 v53, 0\n\t"
      "v_mov_b32 v54, 0\n\t"
      "v_mov_b32 v55, 0\n\t"
      "v_mov_b32 v56, 0\n\t"
      "v_mov_b32 v57, 0\n\t"
      "v_mov_b32 v58, 0\n\t"
      "v_mov_b32 v59, 0\n\t"
      "v_mov_b32 v60, 0\n\t"
      "v_mov_b32 v61, 0\n\t"
      "v_mov_b32 v62, 0\n\t"
      "v_mov_b32 v63, 0\n\t"
      "s_add_u32 m0, s42, 0\n\t"
      "s_nop 0\n\t"
      "buffer_load_dwordx4 v72, %[srdA], s40 offen offset:0 sc1 lds\n\t"
      "buffer_load_dwordx4 v72, %[srdA], s40 offen offset:1024 sc1 lds\n\t"
      "s_add_u32 m0, s42, 2048\n\t"
      "s_nop 0\n\t"
      "buffer_load_dwordx4 v73, %[srdA], s40 offen offset:0 sc1 lds\n\t"
      "buffer_load_dwordx4 v73, %[srdA], s40 offen offset:1024 sc1 lds\n\t"
      "s_add_u32 s40, s40, 2048\n\t"
      "s_add_u32 s42, s42, 32768\n\t"
      "s_add_u32 m0, s42, 0\n\t"
      "s_nop 0\n\t"
      "buffer_load_dwordx4 v72, %[srdA], s40 offen offset:0 sc1 lds\n\t"
      "buffer_load_dwordx4 v72, %[srdA], s40 offen offset:1024 sc1 lds\n\t"
      "s_add_u32 m0, s42, 2048\n\t"
      "s_nop 0\n\t"
      "buffer_load_dwordx4 v73, %[srdA], s40 offen offset:0 sc1 lds\n\t"
      "buffer_load_dwordx4 v73, %[srdA], s40 offen offset:1024 sc1 lds\n\t"
      "s_add_u32 s40, s40, 2048\n\t"
      "s_add_u32 s42, s42, 32768\n\t"
      "s_add_u32 m0, s42, 0\n\t"
      "s_nop 0\n\t"
      "buffer_load_dwordx4 v72, %[srdA], s40 offen offset:0 sc1 lds\n\t"
      "buffer_load_dwordx4 v72, %[srdA], s40 offen offset:1024 sc1 lds\n\t"
      "s_add_u32 m0, s42, 2048\n\t"
      "s_nop 0\n\t"
      "buffer_load_dwordx4 v73, %[srdA], s40 offen offset:0 sc1 lds\n\t"
      "buffer_load_dwordx4 v73, %[srdA], s40 offen offset:1024 sc1 lds\n\t"
      "s_add_u32 s40, s40, 2048\n\t"
      "s_add_u32 s42, s42, 32768\n\t"
      "s_mov_b32 s41, 524288\n\t"
      "s_mov_b32 s42, %[ldsA]\n\t"
      "s_mov_b32 s43, %[ldsW]\n\t"
      "s_mov_b32 s44, 0\n\t"
      "s_mov_b32 s48, 32768\n\t"
      "s_mov_b32 s49, -98304\n\t"
      "s_waitcnt vmcnt(12)\n\t"
      "s_barrier\n\t"
      "ds_read_b64_tr_b16 a[16:17], v65 offset:0\n\t"
      "ds_read_b64_tr_b16 a[18:19], v65 offset:1024\n\t"
      "ds_read_b64_tr_b16 a[20:21], v66 offset:0\n\t"
      "ds_read_b64_tr_b16 a[22:23], v66 offset:1024\n\t"
      "ds_read_b64_tr_b16 a[24:25], v67 offset:0\n\t"
      "ds_read_b64_tr_b16 a[26:27], v67 offset:1024\n\t"
      "ds_read_b64_tr_b16 a[28:29], v68 offset:0\n\t"
      "ds_read_b64_tr_b16 a[30:31], v68 offset:1024\n\t"
      "ds_read_b128 a[0:3], v64 offset:0\n\t"
      "ds_read_b128 a[4:7], v64 offset:2048\n\t"
      "ds_read_b128 a[8:11], v64 offset:4096\n\t"
      "ds_read_b128 a[12:15], v64 offset:6144\n\t"
      "s_waitcnt lgkmcnt(0)\n\t"
      "v_mfma_f32_16x16x32_bf16 v[0:3], a[16:19], a[0:3], v[0:3]\n\t"
      "ds_read_b64_tr_b16 a[48:49], v65 offset:8192\n\t"
      "ds_read_b64_tr_b16 a[50:51], v65 offset:9216\n\t"
      "v_mfma_f32_16x16x32_bf16 v[4:7], a[20:23], a[0:3], v[4:7]\n\t"
      "ds_read_b64_tr_b16 a[52:53], v66 offset:8192\n\t"
      "ds_read_b64_tr_b16 a[54:55], v66 offset:9216\n\t"
      "v_mfma_f32_16x16x32_bf16 v[8:11], a[24:27], a[0:3], v[8:11]\n\t"
      "ds_read_b64_tr_b16 a[56:57], v67 offset:8192\n\t"
      "ds_read_b64_tr_b16 a[58:59], v67 offset:9216\n\t"
      "v_mfma_f32_16x16x32_bf16 v[12:15], a[28:31], a[0:3], v[12:15]\n\t"
      "ds_read_b64_tr_b16 a[60:61], v68 offset:8192\n\t"
      "ds_read_b64_tr_b16 a[62:63], v68 offset:9216\n\t"
      "v_mfma_f32_16x16x32_bf16 v[28:31], a[28:31], a[4:7], v[28:31]\n\t"
      "ds_read_b128 a[32:35], v64 offset:1024\n\t"
      "ds_read_b128 a[36:39], v64 offset:3072\n\t"
      "v_mfma_f32_16x16x32_bf16 v[24:27], a[24:27], a[4:7], v[24:27]\n\t"
      "ds_read_b128 a[40:43], v64 offset:5120\n\t"
      "ds_read_b128 a[44:47], v64 offset:7168\n\t"
      "v_mfma_f32_16x16x32_bf16 v[20:23], a[20:23], a[4:7], v[20:23]\n\t"
      "s_add_u32 s44, s44, 1\n\t"
      "v_mfma_f32_16x16x32_bf16 v[16:19], a[16:19], a[4:7], v[16:19]\n\t"
      "s_cmp_eq_u32 s44, 4\n\t"
      "s_cselect_b32 s46, s49, s48\n\t"
      "s_cselect_b32 s44, 0, s44\n\t"
      "v_mfma_f32_16x16x32_bf16 v[32:35], a[16:19], a[8:11], v[32:35]\n\t"
      "v_add_u32 v64, s46, v64\n\t"
      "v_mfma_f32_16x16x32_bf16 v[36:39], a[20:23], a[8:11], v[36:39]\n\t"
      "v_add_u32 v65, s46, v65\n\t"
      "v_add_u32 v66, s46, v66\n\t"
      "v_mfma_f32_16x16x32_bf16 v[40:43], a[24:27], a[8:11], v[40:43]\n\t"
      "v_add_u32 v67, s46, v67\n\t"
      "v_add_u32 v68, s46, v68\n\t"
      "v_mfma_f32_16x16x32_bf16 v[44:47], a[28:31], a[8:11], v[44:47]\n\t"
      "s_waitcnt vmcnt(8) lgkmcnt(0)\n\t"
      "s_barrier\n\t"
      "v_mfma_f32_16x16x32_bf16 v[60:63], a[28:31], a[12:15], v[60:63]\n\t"
      "s_add_u32 m0, s42, 0\n\t"
      "v_mfma_f32_16x16x32_bf16 v[56:59], a[24:27], a[12:15], v[56:59]\n\t"
      "buffer_load_dwordx4 v72, %[srdA], s40 offen offset:0 sc1 lds\n\t"
      "v_mfma_f32_16x16x32_bf16 v[52:55], a[20:23], a[12:15], v[52:55]\n\t"
      "v_mfma_f32_16x16x32_bf16 v[48:51], a[16:19], a[12:15], v[48:51]\n\t"
      "buffer_load_dwordx4 v72, %[srdA], s40 offen offset:1024 sc1 lds\n\t"
      "v_mfma_f32_16x16x32_bf16 v[0:3], a[48:51], a[32:35], v[0:3]\n\t"
      "ds_read_b64_tr_b16 a[16:17], v65 offset:0\n\t"
      "ds_read_b64_tr_b16 a[18:19], v65 offset:1024\n\t"
      "v_mfma_f32_16x16x32_bf16 v[4:7], a[52:55], a[32:35], v[4:7]\n\t"
      "ds_read_b64_tr_b16 a[20:21], v66 offset:0\n\t"
      "ds_read_b64_tr_b16 a[22:23], v66 offset:1024\n\t"
      "s_add_u32 m0, s42, 2048\n\t"
      "v_mfma_f32_16x16x32_bf16 v[8:11], a[56:59], a[32:35], v[8:11]\n\t"
      "ds_read_b64_tr_b16 a[24:25], v67 offset:0\n\t"
      "ds_read_b64_tr_b16 a[26:27], v67 offset:1024\n\t"
      "buffer_load_dwordx4 v73, %[srdA], s40 offen offset:0 sc1 lds\n\t"
      "v_mfma_f32_16x16x32_bf16 v[12:15], a[60:63], a[32:35], v[12:15]\n\t"
      "ds_read_b64_tr_b16 a[28:29], v68 offset:0\n\t"
      "ds_read_b64_tr_b16 a[30:31], v68 offset:1024\n\t"
      "v_mfma_f32_16x16x32_bf16 v[28:31], a[60:63], a[36:39], v[28:31]\n\t"
      "ds_read_b128 a[0:3], v64 offset:0\n\t"
      "ds_read_b128 a[4:7], v64 offset:2048\n\t"
      "v_mfma_f32_16x16x32_bf16 v[24:27], a[56:59], a[36:39], v[24:27]\n\t"
      "ds_read_b128 a[8:11], v64 offset:4096\n\t"
      "ds_read_b128 a[12:15], v64 offset:6144\n\t"
      "buffer_load_dwordx4 v73, %[srdA], s40 offen offset:1024 sc1 lds\n\t"
      "v_mfma_f32_16x16x32_bf16 v[20:23], a[52:55], a[36:39], v[20:23]\n\t"
      "s_mov_b32 m0, s43\n\t"
      "v_mfma_f32_16x16x32_bf16 v[16:19], a[48:51], a[36:39], v[16:19]\n\t"
      "buffer_load_dwordx4 v74, %[srdW], s41 offen offset:0 lds\n\t"
      "v_mfma_f32_16x16x32_bf16 v[32:35], a[48:51], a[40:43], v[32:35]\n\t"
      "v_mfma_f32_16x16x32_bf16 v[36:39], a[52:55], a[40:43], v[36:39]\n\t"
      "buffer_load_dwordx4 v75, %[srdW], s41 offen offset:1024 lds\n\t"
      "v_mfma_f32_16x16x32_bf16 v[40:43], a[56:59], a[40:43], v[40:43]\n\t"
      "v_mfma_f32_16x16x32_bf16 v[44:47], a[60:63], a[40:43], v[44:47]\n\t"
      "buffer_load_dwordx4 v76, %[srdW], s41 offen offset:2048 lds\n\t"
      "v_mfma_f32_16x16x32_bf16 v[60:63], a[60:63], a[44:47], v[60:63]\n\t"
      "v_mfma_f32_16x16x32_bf16 v[56:59], a[56:59], a[44:47], v[56:59]\n\t"
      "buffer_load_dwordx4 v77, %[srdW], s41 offen offset:3072 lds\n\t"
      "v_mfma_f32_16x16x32_bf16 v[52:55], a[52:55], a[44:47], v[52:55]\n\t"
      "s_add_u32 s40, s40, 2048\n\t"
      "s_add_u32 s41, s41, 131072\n\t"
      "v_mfma_f32_16x16x32_bf16 v[48:51], a[48:51], a[44:47], v[48:51]\n\t"
      "s_waitcnt lgkmcnt(0)\n\t"
      "s_add_u32 s42, s42, s46\n\t"
      "s_add_u32 s43, s43, s46\n\t"
      "v_mfma_f32_16x16x32_bf16 v[0:3], a[16:19], a[0:3], v[0:3]\n\t"
      "ds_read_b64_tr_b16 a[48:49], v65 offset:8192\n\t"
      "ds_read_b64_tr_b16 a[50:51], v65 offset:9216\n\t"
      "v_mfma_f32_16x16x32_bf16 v[4:7], a[20:23], a[0:3], v[4:7]\n\t"
      "ds_read_b64_tr_b16 a[52:53], v66 offset:8192\n\t"
      "ds_read_b64_tr_b16 a[54:55], v66 offset:9216\n\t"
      "v_mfma_f32_16x16x32_bf16 v[8:11], a[24:27], a[0:3], v[8:11]\n\t"
      "ds_read_b64_tr_b16 a[56:57], v67 offset:8192\n\t"
      "ds_read_b64_tr_b16 a[58:59], v67 offset:9216\n\t"
      "v_mfma_f32_16x16x32_bf16 v[12:15], a[28:31], a[0:3], v[12:15]\n\t"
      "ds_read_b64_tr_b16 a[60:61], v68 offset:8192\n\t"
      "ds_read_b64_tr_b16 a[62:63], v68 offset:9216\n\t"
      "v_mfma_f32_16x16x32_bf16 v[28:31], a[28:31], a[4:7], v[28:31]\n\t"
      "ds_read_b128 a[32:35], v64 offset:1024\n\t"
      "ds_read_b128 a[36:39], v64 offset:3072\n\t"
      "v_mfma_f32_16x16x32_bf16 v[24:27], a[24:27], a[4:7], v[24:27]\n\t"
      "ds_read_b128 a[40:43], v64 offset:5120\n\t"
      "ds_read_b128 a[44:47], v64 offset:7168\n\t"
      "v_mfma_f32_16x16x32_bf16 v[20:23], a[20:23], a[4:7], v[20:23]\n\t"
      "s_add_u32 s44, s44, 1\n\t"
      "v_mfma_f32_16x16x32_bf16 v[16:19], a[16:19], a[4:7], v[16:19]\n\t"
      "s_cmp_eq_u32 s44, 4\n\t"
      "s_cselect_b32 s46, s49, s48\n\t"
      "s_cselect_b32 s44, 0, s44\n\t"
      "v_mfma_f32_16x16x32_bf16 v[32:35], a[16:19], a[8:11], v[32:35]\n\t"
      "v_add_u32 v64, s46, v64\n\t"
      "v_mfma_f32_16x16x32_bf16 v[36:39], a[20:23], a[8:11], v[36:39]\n\t"
      "v_add_u32 v65, s46, v65\n\t"
      "v_add_u32 v66, s46, v66\n\t"
      "v_mfma_f32_16x16x32_bf16 v[40:43], a[24:27], a[8:11], v[40:43]\n\t"
      "v_add_u32 v67, s46, v67\n\t"
      "v_add_u32 v68, s46, v68\n\t"
      "v_mfma_f32_16x16x32_bf16 v[44:47], a[28:31], a[8:11], v[44:47]\n\t"
      "s_waitcnt vmcnt(12) lgkmcnt(0)\n\t"
      "s_barrier\n\t"
      "v_mfma_f32_16x16x32_bf16 v[60:63], a[28:31], a[12:15], v[60:63]\n\t"
      "s_add_u32 m0, s42, 0\n\t"
      "v_mfma_f32_16x16x32_bf16 v[56:59], a[24:27], a[12:15], v[56:59]\n\t"
      "buffer_load_dwordx4 v72, %[srdA], s40 offen offset:0 sc1 lds\n\t"
      "v_mfma_f32_16x16x32_bf16 v[52:55], a[20:23], a[12:15], v[52:55]\n\t"
      "v_mfma_f32_16x16x32_bf16 v[48:51], a[16:19], a[12:15], v[48:51]\n\t"
      "buffer_load_dwordx4 v72, %[srdA], s40 offen offset:1024 sc1 lds\n\t"
      "v_mfma_f32_16x16x32_bf16 v[0:3], a[48:51], a[32:35], v[0:3]\n\t"
      "ds_read_b64_tr_b16 a[16:17], v65 offset:0\n\t"
      "ds_read_b64_tr_b16 a[18:19], v65 offset:1024\n\t"
      "v_mfma_f32_16x16x32_bf16 v[4:7], a[52:55], a[32:35], v[4:7]\n\t"
      "ds_read_b64_tr_b16 a[20:21], v66 offset:0\n\t"
      "ds_read_b64_tr_b16 a[22:23], v66 offset:1024\n\t"
      "s_add_u32 m0, s42, 2048\n\t"
      "v_mfma_f32_16x16x32_bf16 v[8:11], a[56:59], a[32:35], v[8:11]\n\t"
      "ds_read_b64_tr_b16 a[24:25], v67 offset:0\n\t"
      "ds_read_b64_tr_b16 a[26:27], v67 offset:1024\n\t"
      "buffer_load_dwordx4 v73, %[srdA], s40 offen offset:0 sc1 lds\n\t"
      "v_mfma_f32_16x16x32_bf16 v[12:15], a[60:63], a[32:35], v[12:15]\n\t"
      "ds_read_b64_tr_b16 a[28:29], v68 offset:0\n\t"
      "ds_read_b64_tr_b16 a[30:31], v68 offset:1024\n\t"
      "v_mfma_f32_16x16x32_bf16 v[28:31], a[60:63], a[36:39], v[28:31]\n\t"
      "ds_read_b128 a[0:3], v64 offset:0\n\t"
      "ds_read_b128 a[4:7], v64 offset:2048\n\t"
      "v_mfma_f32_16x16x32_bf16 v[24:27], a[56:59], a[36:39], v[24:27]\n\t"
      "ds_read_b128 a[8:11], v64 offset:4096\n\t"
      "ds_read_b128 a[12:15], v64 offset:6144\n\t"
      "buffer_load_dwordx4 v73, %[srdA], s40 offen offset:1024 sc1 lds\n\t"
      "v_mfma_f32_16x16x32_bf16 v[20:23], a[52:55], a[36:39], v[20:23]\n\t"
      "s_mov_b32 m0, s43\n\t"
      "v_mfma_f32_16x16x32_bf16 v[16:19], a[48:51], a[36:39], v[16:19]\n\t"
      "buffer_load_dwordx4 v74, %[srdW], s41 offen offset:0 lds\n\t"
      "v_mfma_f32_16x16x32_bf16 v[32:35], a[48:51], a[40:43], v[32:35]\n\t"
      "v_mfma_f32_16x16x32_bf16 v[36:39], a[52:55], a[40:43], v[36:39]\n\t"
      "buffer_load_dwordx4 v75, %[srdW], s41 offen offset:1024 lds\n\t"
      "v_mfma_f32_16x16x32_bf16 v[40:43], a[56:59], a[40:43], v[40:43]\n\t"
      "v_mfma_f32_16x16x32_bf16 v[44:47], a[60:63], a[40:43], v[44:47]\n\t"
      "buffer_load_dwordx4 v76, %[srdW], s41 offen offset:2048 lds\n\t"
      "v_mfma_f32_16x16x32_bf16 v[60:63], a[60:63], a[44:47], v[60:63]\n\t"
      "v_mfma_f32_16x16x32_bf16 v[56:59], a[56:59], a[44:47], v[56:59]\n\t"
      "buffer_load_dwordx4 v77, %[srdW], s41 offen offset:3072 lds\n\t"
      "v_mfma_f32_16x16x32_bf16 v[52:55], a[52:55], a[44:47], v[52:55]\n\t"
      "s_add_u32 s40, s40, 2048\n\t"
      "s_add_u32 s41, s41, 131072\n\t"
      "v_mfma_f32_16x16x32_bf16 v[48:51], a[48:51], a[44:47], v[48:51]\n\t"
      "s_waitcnt lgkmcnt(0)\n\t"
      "s_add_u32 s42, s42, s46\n\t"
      "s_add_u32 s43, s43, s46\n\t"
      "s_mov_b32 s45, 22\n\t"
      "1:\n\t"
      "v_mfma_f32_16x16x32_bf16 v[0:3], a[16:19], a[0:3], v[0:3]\n\t"
      "ds_read_b64_tr_b16 a[48:49], v65 offset:8192\n\t"
      "ds_read_b64_tr_b16 a[50:51], v65 offset:9216\n\t"
      "v_mfma_f32_16x16x32_bf16 v[4:7], a[20:23], a[0:3], v[4:7]\n\t"
      "ds_read_b64_tr_b16 a[52:53], v66 offset:8192\n\t"
      "ds_read_b64_tr_b16 a[54:55], v66 offset:9216\n\t"
      "v_mfma_f32_16x16x32_bf16 v[8:11], a[24:27], a[0:3], v[8:11]\n\t"
      "ds_read_b64_tr_b16 a[56:57], v67 offset:8192\n\t"
      "ds_read_b64_tr_b16 a[58:59], v67 offset:9216\n\t"
      "v_mfma_f32_16x16x32_bf16 v[12:15], a[28:31], a[0:3], v[12:15]\n\t"
      "ds_read_b64_tr_b16 a[60:61], v68 offset:8192\n\t"
      "ds_read_b64_tr_b16 a[62:63], v68 offset:9216\n\t"
      "v_mfma_f32_16x16x32_bf16 v[28:31], a[28:31], a[4:7], v[28:31]\n\t"
      "ds_read_b128 a[32:35], v64 offset:1024\n\t"
      "ds_read_b128 a[36:39], v64 offset:3072\n\t"
      "v_mfma_f32_16x16x32_bf16 v[24:27], a[24:27], a[4:7], v[24:27]\n\t"
      "ds_read_b128 a[40:43], v64 offset:5120\n\t"
      "ds_read_b128 a[44:47], v64 offset:7168\n\t"
      "v_mfma_f32_16x16x32_bf16 v[20:23], a[20:23], a[4:7], v[20:23]\n\t"
      "s_add_u32 s44, s44, 1\n\t"
      "v_mfma_f32_16x16x32_bf16 v[16:19], a[16:19], a[4:7], v[16:19]\n\t"
      "s_cmp_eq_u32 s44, 4\n\t"
      "s_cselect_b32 s46, s49, s48\n\t"
      "s_cselect_b32 s44, 0, s44\n\t"
      "v_mfma_f32_16x16x32_bf16 v[32:35], a[16:19], a[8:11], v[32:35]\n\t"
      "v_add_u32 v64, s46, v64\n\t"
      "v_mfma_f32_16x16x32_bf16 v[36:39], a[20:23], a[8:11], v[36:39]\n\t"
      "v_add_u32 v65, s46, v65\n\t"
      "v_add_u32 v66, s46, v66\n\t"
      "v_mfma_f32_16x16x32_bf16 v[40:43], a[24:27], a[8:11], v[40:43]\n\t"
      "v_add_u32 v67, s46, v67\n\t"
      "v_add_u32 v68, s46, v68\n\t"
      "v_mfma_f32_16x16x32_bf16 v[44:47], a[28:31], a[8:11], v[44:47]\n\t"
      "s_waitcnt vmcnt(16) lgkmcnt(0)\n\t"
      "s_barrier\n\t"
      "v_mfma_f32_16x16x32_bf16 v[60:63], a[28:31], a[12:15], v[60:63]\n\t"
      "s_add_u32 m0, s42, 0\n\t"
      "v_mfma_f32_16x16x32_bf16 v[56:59], a[24:27], a[12:15], v[56:59]\n\t"
      "buffer_load_dwordx4 v72, %[srdA], s40 offen offset:0 sc1 lds\n\t"
      "v_mfma_f32_16x16x32_bf16 v[52:55], a[20:23], a[12:15], v[52:55]\n\t"
      "v_mfma_f32_16x16x32_bf16 v[48:51], a[16:19], a[12:15], v[48:51]\n\t"
      "buffer_load_dwordx4 v72, %[srdA], s40 offen offset:1024 sc1 lds\n\t"
      "v_mfma_f32_16x16x32_bf16 v[0:3], a[48:51], a[32:35], v[0:3]\n\t"
      "ds_read_b64_tr_b16 a[16:17], v65 offset:0\n\t"
      "ds_read_b64_tr_b16 a[18:19], v65 offset:1024\n\t"
      "v_mfma_f32_16x16x32_bf16 v[4:7], a[52:55], a[32:35], v[4:7]\n\t"
      "ds_read_b64_tr_b16 a[20:21], v66 offset:0\n\t"
      "ds_read_b64_tr_b16 a[22:23], v66 offset:1024\n\t"
      "s_add_u32 m0, s42, 2048\n\t"
      "v_mfma_f32_16x16x32_bf16 v[8:11], a[56:59], a[32:35], v[8:11]\n\t"
      "ds_read_b64_tr_b16 a[24:25], v67 offset:0\n\t"
      "ds_read_b64_tr_b16 a[26:27], v67 offset:1024\n\t"
      "buffer_load_dwordx4 v73, %[srdA], s40 offen offset:0 sc1 lds\n\t"
      "v_mfma_f32_16x16x32_bf16 v[12:15], a[60:63], a[32:35], v[12:15]\n\t"
      "ds_read_b64_tr_b16 a[28:29], v68 offset:0\n\t"
      "ds_read_b64_tr_b16 a[30:31], v68 offset:1024\n\t"
      "v_mfma_f32_16x16x32_bf16 v[28:31], a[60:63], a[36:39], v[28:31]\n\t"
      "ds_read_b128 a[0:3], v64 offset:0\n\t"
      "ds_read_b128 a[4:7], v64 offset:2048\n\t"
      "v_mfma_f32_16x16x32_bf16 v[24:27], a[56:59], a[36:39], v[24:27]\n\t"
      "ds_read_b128 a[8:11], v64 offset:4096\n\t"
      "ds_read_b128 a[12:15], v64 offset:6144\n\t"
      "buffer_load_dwordx4 v73, %[srdA], s40 offen offset:1024 sc1 lds\n\t"
      "v_mfma_f32_16x16x32_bf16 v[20:23], a[52:55], a[36:39], v[20:23]\n\t"
      "s_mov_b32 m0, s43\n\t"
      "v_mfma_f32_16x16x32_bf16 v[16:19], a[48:51], a[36:39], v[16:19]\n\t"
      "buffer_load_dwordx4 v74, %[srdW], s41 offen offset:0 lds\n\t"
      "v_mfma_f32_16x16x32_bf16 v[32:35], a[48:51], a[40:43], v[32:35]\n\t"
      "v_mfma_f32_16x16x32_bf16 v[36:39], a[52:55], a[40:43], v[36:39]\n\t"
      "buffer_load_dwordx4 v75, %[srdW], s41 offen offset:1024 lds\n\t"
      "v_mfma_f32_16x16x32_bf16 v[40:43], a[56:59], a[40:43], v[40:43]\n\t"
      "v_mfma_f32_16x16x32_bf16 v[44:47], a[60:63], a[40:43], v[44:47]\n\t"
      "buffer_load_dwordx4 v76, %[srdW], s41 offen offset:2048 lds\n\t"
      "v_mfma_f32_16x16x32_bf16 v[60:63], a[60:63], a[44:47], v[60:63]\n\t"
      "v_mfma_f32_16x16x32_bf16 v[56:59], a[56:59], a[44:47], v[56:59]\n\t"
      "buffer_load_dwordx4 v77, %[srdW], s41 offen offset:3072 lds\n\t"
      "v_mfma_f32_16x16x32_bf16 v[52:55], a[52:55], a[44:47], v[52:55]\n\t"
      "s_add_u32 s40, s40, 2048\n\t"
      "s_add_u32 s41, s41, 131072\n\t"
      "v_mfma_f32_16x16x32_bf16 v[48:51], a[48:51], a[44:47], v[48:51]\n\t"
      "s_waitcnt lgkmcnt(0)\n\t"
      "s_add_u32 s42, s42, s46\n\t"
      "s_add_u32 s43, s43, s46\n\t"
      "s_sub_u32 s45, s45, 1\n\t"
      "s_cmp_lg_u32 s45, 0\n\t"
      "s_cbranch_scc1 1b\n\t"
      "v_mfma_f32_16x16x32_bf16 v[0:3], a[16:19], a[0:3], v[0:3]\n\t"
      "ds_read_b64_tr_b16 a[48:49], v65 offset:8192\n\t"
      "ds_read_b64_tr_b16 a[50:51], v65 offset:9216\n\t"
      "v_mfma_f32_16x16x32_bf16 v[4:7], a[20:23], a[0:3], v[4:7]\n\t"
      "ds_read_b64_tr_b16 a[52:53], v66 offset:8192\n\t"
      "ds_read_b64_tr_b16 a[54:55], v66 offset:9216\n\t"
      "v_mfma_f32_16x16x32_bf16 v[8:11], a[24:27], a[0:3], v[8:11]\n\t"
      "ds_read_b64_tr_b16 a[56:57], v67 offset:8192\n\t"
      "ds_read_b64_tr_b16 a[58:59], v67 offset:9216\n\t"
      "v_mfma_f32_16x16x32_bf16 v[12:15], a[28:31], a[0:3], v[12:15]\n\t"
      "ds_read_b64_tr_b16 a[60:61], v68 offset:8192\n\t"
      "ds_read_b64_tr_b16 a[62:63], v68 offset:9216\n\t"
      "v_mfma_f32_16x16x32_bf16 v[28:31], a[28:31], a[4:7], v[28:31]\n\t"
      "ds_read_b128 a[32:35], v64 offset:1024\n\t"
      "ds_read_b128 a[36:39], v64 offset:3072\n\t"
      "v_mfma_f32_16x16x32_bf16 v[24:27], a[24:27], a[4:7], v[24:27]\n\t"
      "ds_read_b128 a[40:43], v64 offset:5120\n\t"
      "ds_read_b128 a[44:47], v64 offset:7168\n\t"
      "v_mfma_f32_16x16x32_bf16 v[20:23], a[20:23], a[4:7], v[20:23]\n\t"
      "s_add_u32 s44, s44, 1\n\t"
      "v_mfma_f32_16x16x32_bf16 v[16:19], a[16:19], a[4:7], v[16:19]\n\t"
      "s_cmp_eq_u32 s44, 4\n\t"
      "s_cselect_b32 s46, s49, s48\n\t"
      "s_cselect_b32 s44, 0, s44\n\t"
      "v_mfma_f32_16x16x32_bf16 v[32:35], a[16:19], a[8:11], v[32:35]\n\t"
      "v_add_u32 v64, s46, v64\n\t"
      "v_mfma_f32_16x16x32_bf16 v[36:39], a[20:23], a[8:11], v[36:39]\n\t"
      "v_add_u32 v65, s46, v65\n\t"
      "v_add_u32 v66, s46, v66\n\t"
      "v_mfma_f32_16x16x32_bf16 v[40:43], a[24:27], a[8:11], v[40:43]\n\t"
      "v_add_u32 v67, s46, v67\n\t"
      "v_add_u32 v68, s46, v68\n\t"
      "v_mfma_f32_16x16x32_bf16 v[44:47], a[28:31], a[8:11], v[44:47]\n\t"
      "s_waitcnt vmcnt(16) lgkmcnt(0)\n\t"
      "s_barrier\n\t"
      "v_mfma_f32_16x16x32_bf16 v[60:63], a[28:31], a[12:15], v[60:63]\n\t"
      "v_mfma_f32_16x16x32_bf16 v[56:59], a[24:27], a[12:15], v[56:59]\n\t"
      "v_mfma_f32_16x16x32_bf16 v[52:55], a[20:23], a[12:15], v[52:55]\n\t"
      "v_mfma_f32_16x16x32_bf16 v[48:51], a[16:19], a[12:15], v[48:51]\n\t"
      "v_mfma_f32_16x16x32_bf16 v[0:3], a[48:51], a[32:35], v[0:3]\n\t"
      "ds_read_b64_tr_b16 a[16:17], v65 offset:0\n\t"
      "ds_read_b64_tr_b16 a[18:19], v65 offset:1024\n\t"
      "v_mfma_f32_16x16x32_bf16 v[4:7], a[52:55], a[32:35], v[4:7]\n\t"
      "ds_read_b64_tr_b16 a[20:21], v66 offset:0\n\t"
      "ds_read_b64_tr_b16 a[22:23], v66 offset:1024\n\t"
      "v_mfma_f32_16x16x32_bf16 v[8:11], a[56:59], a[32:35], v[8:11]\n\t"
      "ds_read_b64_tr_b16 a[24:25], v67 offset:0\n\t"
      "ds_read_b64_tr_b16 a[26:27], v67 offset:1024\n\t"
      "v_mfma_f32_16x16x32_bf16 v[12:15], a[60:63], a[32:35], v[12:15]\n\t"
      "ds_read_b64_tr_b16 a[28:29], v68 offset:0\n\t"
      "ds_read_b64_tr_b16 a[30:31], v68 offset:1024\n\t"
      "v_mfma_f32_16x16x32_bf16 v[28:31], a[60:63], a[36:39], v[28:31]\n\t"
      "ds_read_b128 a[0:3], v64 offset:0\n\t"
      "ds_read_b128 a[4:7], v64 offset:2048\n\t"
      "v_mfma_f32_16x16x32_bf16 v[24:27], a[56:59], a[36:39], v[24:27]\n\t"
      "ds_read_b128 a[8:11], v64 offset:4096\n\t"
      "ds_read_b128 a[12:15], v64 offset:6144\n\t"
      "v_mfma_f32_16x16x32_bf16 v[20:23], a[52:55], a[36:39], v[20:23]\n\t"
      "v_mfma_f32_16x16x32_bf16 v[16:19], a[48:51], a[36:39], v[16:19]\n\t"
      "v_mfma_f32_16x16x32_bf16 v[32:35], a[48:51], a[40:43], v[32:35]\n\t"
      "v_mfma_f32_16x16x32_bf16 v[36:39], a[52:55], a[40:43], v[36:39]\n\t"
      "v_mfma_f32_16x16x32_bf16 v[40:43], a[56:59], a[40:43], v[40:43]\n\t"
      "v_mfma_f32_16x16x32_bf16 v[44:47], a[60:63], a[40:43], v[44:47]\n\t"
      "v_mfma_f32_16x16x32_bf16 v[60:63], a[60:63], a[44:47], v[60:63]\n\t"
      "v_mfma_f32_16x16x32_bf16 v[56:59], a[56:59], a[44:47], v[56:59]\n\t"
      "v_mfma_f32_16x16x32_bf16 v[52:55], a[52:55], a[44:47], v[52:55]\n\t"
      "v_mfma_f32_16x16x32_bf16 v[48:51], a[48:51], a[44:47], v[48:51]\n\t"
      "s_waitcnt lgkmcnt(0)\n\t"
      "v_mfma_f32_16x16x32_bf16 v[0:3], a[16:19], a[0:3], v[0:3]\n\t"
      "ds_read_b64_tr_b16 a[48:49], v65 offset:8192\n\t"
      "ds_read_b64_tr_b16 a[50:51], v65 offset:9216\n\t"
      "v_mfma_f32_16x16x32_bf16 v[4:7], a[20:23], a[0:3], v[4:7]\n\t"
      "ds_read_b64_tr_b16 a[52:53], v66 offset:8192\n\t"
      "ds_read_b64_tr_b16 a[54:55], v66 offset:9216\n\t"
      "v_mfma_f32_16x16x32_bf16 v[8:11], a[24:27], a[0:3], v[8:11]\n\t"
      "ds_read_b64_tr_b16 a[56:57], v67 offset:8192\n\t"
      "ds_read_b64_tr_b16 a[58:59], v67 offset:9216\n\t"
      "v_mfma_f32_16x16x32_bf16 v[12:15], a[28:31], a[0:3], v[12:15]\n\t"
      "ds_read_b64_tr_b16 a[60:61], v68 offset:8192\n\t"
      "ds_read_b64_tr_b16 a[62:63], v68 offset:9216\n\t"
      "v_mfma_f32_16x16x32_bf16 v[28:31], a[28:31], a[4:7], v[28:31]\n\t"
      "ds_read_b128 a[32:35], v64 offset:1024\n\t"
      "ds_read_b128 a[36:39], v64 offset:3072\n\t"
      "v_mfma_f32_16x16x32_bf16 v[24:27], a[24:27], a[4:7], v[24:27]\n\t"
      "ds_read_b128 a[40:43], v64 offset:5120\n\t"
      "ds_read_b128 a[44:47], v64 offset:7168\n\t"
      "v_mfma_f32_16x16x32_bf16 v[20:23], a[20:23], a[4:7], v[20:23]\n\t"
      "s_add_u32 s44, s44, 1\n\t"
      "v_mfma_f32_16x16x32_bf16 v[16:19], a[16:19], a[4:7], v[16:19]\n\t"
      "s_cmp_eq_u32 s44, 4\n\t"
      "s_cselect_b32 s46, s49, s48\n\t"
      "s_cselect_b32 s44, 0, s44\n\t"
      "v_mfma_f32_16x16x32_bf16 v[32:35], a[16:19], a[8:11], v[32:35]\n\t"
      "v_add_u32 v64, s46, v64\n\t"
      "v_mfma_f32_16x16x32_bf16 v[36:39], a[20:23], a[8:11], v[36:39]\n\t"
      "v_add_u32 v65, s46, v65\n\t"
      "v_add_u32 v66, s46, v66\n\t"
      "v_mfma_f32_16x16x32_bf16 v[40:43], a[24:27], a[8:11], v[40:43]\n\t"
      "v_add_u32 v67, s46, v67\n\t"
      "v_add_u32 v68, s46, v68\n\t"
      "v_mfma_f32_16x16x32_bf16 v[44:47], a[28:31], a[8:11], v[44:47]\n\t"
      "s_waitcnt vmcnt(8) lgkmcnt(0)\n\t"
      "s_barrier\n\t"
      "v_mfma_f32_16x16x32_bf16 v[60:63], a[28:31], a[12:15], v[60:63]\n\t"
      "v_mfma_f32_16x16x32_bf16 v[56:59], a[24:27], a[12:15], v[56:59]\n\t"
      "v_mfma_f32_16x16x32_bf16 v[52:55], a[20:23], a[12:15], v[52:55]\n\t"
      "v_mfma_f32_16x16x32_bf16 v[48:51], a[16:19], a[12:15], v[48:51]\n\t"
      "v_mfma_f32_16x16x32_bf16 v[0:3], a[48:51], a[32:35], v[0:3]\n\t"
      "ds_read_b64_tr_b16 a[16:17], v65 offset:0\n\t"
      "ds_read_b64_tr_b16 a[18:19], v65 offset:1024\n\t"
      "v_mfma_f32_16x16x32_bf16 v[4:7], a[52:55], a[32:35], v[4:7]\n\t"
      "ds_read_b64_tr_b16 a[20:21], v66 offset:0\n\t"
      "ds_read_b64_tr_b16 a[22:23], v66 offset:1024\n\t"
      "v_mfma_f32_16x16x32_bf16 v[8:11], a[56:59], a[32:35], v[8:11]\n\t"
      "ds_read_b64_tr_b16 a[24:25], v67 offset:0\n\t"
      "ds_read_b64_tr_b16 a[26:27], v67 offset:1024\n\t"
      "v_mfma_f32_16x16x32_bf16 v[12:15], a[60:63], a[32:35], v[12:15]\n\t"
      "ds_read_b64_tr_b16 a[28:29], v68 offset:0\n\t"
      "ds_read_b64_tr_b16 a[30:31], v68 offset:1024\n\t"
      "v_mfma_f32_16x16x32_bf16 v[28:31], a[60:63], a[36:39], v[28:31]\n\t"
      "ds_read_b128 a[0:3], v64 offset:0\n\t"
      "ds_read_b128 a[4:7], v64 offset:2048\n\t"
      "v_mfma_f32_16x16x32_bf16 v[24:27], a[56:59], a[36:39], v[24:27]\n\t"
      "ds_read_b128 a[8:11], v64 offset:4096\n\t"
      "ds_read_b128 a[12:15], v64 offset:6144\n\t"
      "v_mfma_f32_16x16x32_bf16 v[20:23], a[52:55], a[36:39], v[20:23]\n\t"
      "v_mfma_f32_16x16x32_bf16 v[16:19], a[48:51], a[36:39], v[16:19]\n\t"
      "v_mfma_f32_16x16x32_bf16 v[32:35], a[48:51], a[40:43], v[32:35]\n\t"
      "v_mfma_f32_16x16x32_bf16 v[36:39], a[52:55], a[40:43], v[36:39]\n\t"
      "v_mfma_f32_16x16x32_bf16 v[40:43], a[56:59], a[40:43], v[40:43]\n\t"
      "v_mfma_f32_16x16x32_bf16 v[44:47], a[60:63], a[40:43], v[44:47]\n\t"
      "v_mfma_f32_16x16x32_bf16 v[60:63], a[60:63], a[44:47], v[60:63]\n\t"
      "v_mfma_f32_16x16x32_bf16 v[56:59], a[56:59], a[44:47], v[56:59]\n\t"
      "v_mfma_f32_16x16x32_bf16 v[52:55], a[52:55], a[44:47], v[52:55]\n\t"
      "v_mfma_f32_16x16x32_bf16 v[48:51], a[48:51], a[44:47], v[48:51]\n\t"
      "s_waitcnt lgkmcnt(0)\n\t"
      "v_mfma_f32_16x16x32_bf16 v[0:3], a[16:19], a[0:3], v[0:3]\n\t"
      "ds_read_b64_tr_b16 a[48:49], v65 offset:8192\n\t"
      "ds_read_b64_tr_b16 a[50:51], v65 offset:9216\n\t"
      "v_mfma_f32_16x16x32_bf16 v[4:7], a[20:23], a[0:3], v[4:7]\n\t"
      "ds_read_b64_tr_b16 a[52:53], v66 offset:8192\n\t"
      "ds_read_b64_tr_b16 a[54:55], v66 offset:9216\n\t"
      "v_mfma_f32_16x16x32_bf16 v[8:11], a[24:27], a[0:3], v[8:11]\n\t"
      "ds_read_b64_tr_b16 a[56:57], v67 offset:8192\n\t"
      "ds_read_b64_tr_b16 a[58:59], v67 offset:9216\n\t"
      "v_mfma_f32_16x16x32_bf16 v[12:15], a[28:31], a[0:3], v[12:15]\n\t"
      "ds_read_b64_tr_b16 a[60:61], v68 offset:8192\n\t"
      "ds_read_b64_tr_b16 a[62:63], v68 offset:9216\n\t"
      "v_mfma_f32_16x16x32_bf16 v[28:31], a[28:31], a[4:7], v[28:31]\n\t"
      "ds_read_b128 a[32:35], v64 offset:1024\n\t"
      "ds_read_b128 a[36:39], v64 offset:3072\n\t"
      "v_mfma_f32_16x16x32_bf16 v[24:27], a[24:27], a[4:7], v[24:27]\n\t"
      "ds_read_b128 a[40:43], v64 offset:5120\n\t"
      "ds_read_b128 a[44:47], v64 offset:7168\n\t"
      "v_mfma_f32_16x16x32_bf16 v[20:23], a[20:23], a[4:7], v[20:23]\n\t"
      "s_add_u32 s44, s44, 1\n\t"
      "v_mfma_f32_16x16x32_bf16 v[16:19], a[16:19], a[4:7], v[16:19]\n\t"
      "s_cmp_eq_u32 s44, 4\n\t"
      "s_cselect_b32 s46, s49, s48\n\t"
      "s_cselect_b32 s44, 0, s44\n\t"
      "v_mfma_f32_16x16x32_bf16 v[32:35], a[16:19], a[8:11], v[32:35]\n\t"
      "v_add_u32 v64, s46, v64\n\t"
      "v_mfma_f32_16x16x32_bf16 v[36:39], a[20:23], a[8:11], v[36:39]\n\t"
      "v_add_u32 v65, s46, v65\n\t"
      "v_add_u32 v66, s46, v66\n\t"
      "v_mfma_f32_16x16x32_bf16 v[40:43], a[24:27], a[8:11], v[40:43]\n\t"
      "v_add_u32 v67, s46, v67\n\t"
      "v_add_u32 v68, s46, v68\n\t"
      "v_mfma_f32_16x16x32_bf16 v[44:47], a[28:31], a[8:11], v[44:47]\n\t"
      "s_waitcnt vmcnt(0) lgkmcnt(0)\n\t"
      "s_barrier\n\t"
      "v_mfma_f32_16x16x32_bf16 v[60:63], a[28:31], a[12:15], v[60:63]\n\t"
      "v_mfma_f32_16x16x32_bf16 v[56:59], a[24:27], a[12:15], v[56:59]\n\t"
      "v_mfma_f32_16x16x32_bf16 v[52:55], a[20:23], a[12:15], v[52:55]\n\t"
      "v_mfma_f32_16x16x32_bf16 v[48:51], a[16:19], a[12:15], v[48:51]\n\t"
      "v_mfma_f32_16x16x32_bf16 v[0:3], a[48:51], a[32:35], v[0:3]\n\t"
      "ds_read_b64_tr_b16 a[16:17], v65 offset:0\n\t"
      "ds_read_b64_tr_b16 a[18:19], v65 offset:1024\n\t"
      "v_mfma_f32_16x16x32_bf16 v[4:7], a[52:55], a[32:35], v[4:7]\n\t"
      "ds_read_b64_tr_b16 a[20:21], v66 offset:0\n\t"
      "ds_read_b64_tr_b16 a[22:23], v66 offset:1024\n\t"
      "v_mfma_f32_16x16x32_bf16 v[8:11], a[56:59], a[32:35], v[8:11]\n\t"
      "ds_read_b64_tr_b16 a[24:25], v67 offset:0\n\t"
      "ds_read_b64_tr_b16 a[26:27], v67 offset:1024\n\t"
      "v_mfma_f32_16x16x32_bf16 v[12:15], a[60:63], a[32:35], v[12:15]\n\t"
      "ds_read_b64_tr_b16 a[28:29], v68 offset:0\n\t"
      "ds_read_b64_tr_b16 a[30:31], v68 offset:1024\n\t"
      "v_mfma_f32_16x16x32_bf16 v[28:31], a[60:63], a[36:39], v[28:31]\n\t"
      "ds_read_b128 a[0:3], v64 offset:0\n\t"
      "ds_read_b128 a[4:7], v64 offset:2048\n\t"
      "v_mfma_f32_16x16x32_bf16 v[24:27], a[56:59], a[36:39], v[24:27]\n\t"
      "ds_read_b128 a[8:11], v64 offset:4096\n\t"
      "ds_read_b128 a[12:15], v64 offset:6144\n\t"
      "v_mfma_f32_16x16x32_bf16 v[20:23], a[52:55], a[36:39], v[20:23]\n\t"
      "v_mfma_f32_16x16x32_bf16 v[16:19], a[48:51], a[36:39], v[16:19]\n\t"
      "v_mfma_f32_16x16x32_bf16 v[32:35], a[48:51], a[40:43], v[32:35]\n\t"
      "v_mfma_f32_16x16x32_bf16 v[36:39], a[52:55], a[40:43], v[36:39]\n\t"
      "v_mfma_f32_16x16x32_bf16 v[40:43], a[56:59], a[40:43], v[40:43]\n\t"
      "v_mfma_f32_16x16x32_bf16 v[44:47], a[60:63], a[40:43], v[44:47]\n\t"
      "v_mfma_f32_16x16x32_bf16 v[60:63], a[60:63], a[44:47], v[60:63]\n\t"
      "v_mfma_f32_16x16x32_bf16 v[56:59], a[56:59], a[44:47], v[56:59]\n\t"
      "v_mfma_f32_16x16x32_bf16 v[52:55], a[52:55], a[44:47], v[52:55]\n\t"
      "v_mfma_f32_16x16x32_bf16 v[48:51], a[48:51], a[44:47], v[48:51]\n\t"
      "s_waitcnt lgkmcnt(0)\n\t"
      "v_mfma_f32_16x16x32_bf16 v[0:3], a[16:19], a[0:3], v[0:3]\n\t"
      "ds_read_b64_tr_b16 a[48:49], v65 offset:8192\n\t"
      "ds_read_b64_tr_b16 a[50:51], v65 offset:9216\n\t"
      "v_mfma_f32_16x16x32_bf16 v[4:7], a[20:23], a[0:3], v[4:7]\n\t"
      "ds_read_b64_tr_b16 a[52:53], v66 offset:8192\n\t"
      "ds_read_b64_tr_b16 a[54:55], v66 offset:9216\n\t"
      "v_mfma_f32_16x16x32_bf16 v[8:11], a[24:27], a[0:3], v[8:11]\n\t"
      "ds_read_b64_tr_b16 a[56:57], v67 offset:8192\n\t"
      "ds_read_b64_tr_b16 a[58:59], v67 offset:9216\n\t"
      "v_mfma_f32_16x16x32_bf16 v[12:15], a[28:31], a[0:3], v[12:15]\n\t"
      "ds_read_b64_tr_b16 a[60:61], v68 offset:8192\n\t"
      "ds_read_b64_tr_b16 a[62:63], v68 offset:9216\n\t"
      "v_mfma_f32_16x16x32_bf16 v[28:31], a[28:31], a[4:7], v[28:31]\n\t"
      "ds_read_b128 a[32:35], v64 offset:1024\n\t"
      "ds_read_b128 a[36:39], v64 offset:3072\n\t"
      "v_mfma_f32_16x16x32_bf16 v[24:27], a[24:27], a[4:7], v[24:27]\n\t"
      "ds_read_b128 a[40:43], v64 offset:5120\n\t"
      "ds_read_b128 a[44:47], v64 offset:7168\n\t"
      "v_mfma_f32_16x16x32_bf16 v[20:23], a[20:23], a[4:7], v[20:23]\n\t"
      "v_mfma_f32_16x16x32_bf16 v[16:19], a[16:19], a[4:7], v[16:19]\n\t"
      "v_mfma_f32_16x16x32_bf16 v[32:35], a[16:19], a[8:11], v[32:35]\n\t"
      "v_mfma_f32_16x16x32_bf16 v[36:39], a[20:23], a[8:11], v[36:39]\n\t"
      "v_mfma_f32_16x16x32_bf16 v[40:43], a[24:27], a[8:11], v[40:43]\n\t"
      "v_mfma_f32_16x16x32_bf16 v[44:47], a[28:31], a[8:11], v[44:47]\n\t"
      "v_mfma_f32_16x16x32_bf16 v[60:63], a[28:31], a[12:15], v[60:63]\n\t"
      "v_mfma_f32_16x16x32_bf16 v[56:59], a[24:27], a[12:15], v[56:59]\n\t"
      "v_mfma_f32_16x16x32_bf16 v[52:55], a[20:23], a[12:15], v[52:55]\n\t"
      "v_mfma_f32_16x16x32_bf16 v[48:51], a[16:19], a[12:15], v[48:51]\n\t"
      "s_waitcnt lgkmcnt(0)\n\t"
      "v_mfma_f32_16x16x32_bf16 v[0:3], a[48:51], a[32:35], v[0:3]\n\t"
      "v_mfma_f32_16x16x32_bf16 v[4:7], a[52:55], a[32:35], v[4:7]\n\t"
      "v_mfma_f32_16x16x32_bf16 v[8:11], a[56:59], a[32:35], v[8:11]\n\t"
      "v_mfma_f32_16x16x32_bf16 v[12:15], a[60:63], a[32:35], v[12:15]\n\t"
      "v_mfma_f32_16x16x32_bf16 v[28:31], a[60:63], a[36:39], v[28:31]\n\t"
      "v_mfma_f32_16x16x32_bf16 v[24:27], a[56:59], a[36:39], v[24:27]\n\t"
      "v_mfma_f32_16x16x32_bf16 v[20:23], a[52:55], a[36:39], v[20:23]\n\t"
      "v_mfma_f32_16x16x32_bf16 v[16:19], a[48:51], a[36:39], v[16:19]\n\t"
      "v_mfma_f32_16x16x32_bf16 v[32:35], a[48:51], a[40:43], v[32:35]\n\t"
      "v_mfma_f32_16x16x32_bf16 v[36:39], a[52:55], a[40:43], v[36:39]\n\t"
      "v_mfma_f32_16x16x32_bf16 v[40:43], a[56:59], a[40:43], v[40:43]\n\t"
      "v_mfma_f32_16x16x32_bf16 v[44:47], a[60:63], a[40:43], v[44:47]\n\t"
      "v_mfma_f32_16x16x32_bf16 v[60:63], a[60:63], a[44:47], v[60:63]\n\t"
      "v_mfma_f32_16x16x32_bf16 v[56:59], a[56:59], a[44:47], v[56:59]\n\t"
      "v_mfma_f32_16x16x32_bf16 v[52:55], a[52:55], a[44:47], v[52:55]\n\t"
      "v_mfma_f32_16x16x32_bf16 v[48:51], a[48:51], a[44:47], v[48:51]\n\t"
      "s_nop 7\n\t"
      "s_nop 7\n\t"
      : "={v[0:31]}"(A0), "={v[32:63]}"(A1), "+{v[64:71]}"(r0)
      : "{v[72:79]}"(vd), [srdA] "s"(srdA), [srdW] "s"(srdW), [ldsA] "s"(ldsA), [ldsW] "s"(ldsW)
      : "memory", "scc", "a0", "a1", "a2", "a3", "a4", "a5", "a6", "a7", "a8", "a9", "a10", "a11", "a12", "a13", "a14", "a15", "a16", "a17", "a18", "a19", "a20", "a21", "a22", "a23", "a24", "a25", "a26", "a27", "a28", "a29", "a30", "a31", "a32", "a33", "a34", "a35", "a36", "a37", "a38", "a39", "a40", "a41", "a42", "a43", "a44", "a45", "a46", "a47", "a48", "a49", "a50", "a51", "a52", "a53", "a54", "a55", "a56", "a57", "a58", "a59", "a60", "a61", "a62", "a63", "s40", "s41", "s42", "s43", "s44", "s45", "s46", "s47", "s48", "s49");
    const f32x32 mine = kh ? A1 : A0, send = kh ? A0 : A1;
    {
      const auto srdX = mk_rsrc((char*)xbuf + (size_t)(tile * 2 + kh) * 16384, 16384);
#pragma unroll
      for (int j = 0; j < 4; ++j) {
        u32x4 w = {pk2(send[8 * j], send[8 * j + 1]), pk2(send[8 * j + 2], send[8 * j + 3]), pk2(send[8 * j + 4], send[8 * j + 5]), pk2(send[8 * j + 6], send[8 * j + 7])};
#pragma unroll
        for (int e = 0; e < 4; ++e) w[e] = min(w[e], 0xfffffffeu);
        __builtin_amdgcn_raw_buffer_store_b128(w, srdX, wid * 4096 + j * 1024 + lane * 16, 0, 16  );
      }
    }
    u32x2 hv[8];
    size_t obase = (size_t)(t2 + wm * 64 + 32 * kh + c) * D_ + d0 + wn * 64 + 4 * q;
    {
      const auto srdR = mk_rsrc((const char*)h + (size_t)t2 * D_ * 2, 128 * D_ * 2);
      const u32 ro = ((wm * 64 + 32 * kh + c) * D_ + d0 + wn * 64 + 4 * q) * 2;
#pragma unroll
      for (int j = 0; j < 8; ++j) hv[j] = __builtin_amdgcn_raw_buffer_load_b64(srdR, ro + (j >> 2) * 16 * D_ * 2 + (j & 3) * 32, 0, 16  );
    }
    {
      const auto srdX = mk_rsrc((char*)xbuf + (size_t)(tile * 2 + (kh ^ 1)) * 16384, 16384);
      u32x4 pv[4];
      for (int spin = 0; spin < (1 << 16); ++spin) {
#pragma unroll
        for (int j = 0; j < 4; ++j) pv[j] = __builtin_amdgcn_raw_buffer_load_b128(srdX, wid * 4096 + j * 1024 + lane * 16, 0, 16  );
        u32 m = 0;
#pragma unroll
        for (int j = 0; j < 4; ++j) m = max(max(m, max(pv[j][0], pv[j][1])), max(pv[j][2], pv[j][3]));
        if (__builtin_amdgcn_ballot_w64(m == 0xffffffffu) == 0) break;
        __builtin_amdgcn_s_sleep(2);
      }
#pragma unroll
      for (int j = 0; j < 8; ++j) {
        const u32 p01 = pv[j >> 1][(j & 1) * 2], p23 = pv[j >> 1][(j & 1) * 2 + 1];
        const float s0 = mine[4 * j] + lo16(p01), s1 = mine[4 * j + 1] + hi16(p01), s2 = mine[4 * j + 2] + lo16(p23), s3 = mine[4 * j + 3] + hi16(p23);
        const u32 a01 = pk2(s0, s1), a23 = pk2(s2, s3);
        *(u32x2*)(out + obase + (size_t)(j >> 2) * 16 * D_ + (j & 3) * 16) = u32x2{pk2(lo16(hv[j][0]) + lo16(a01), hi16(hv[j][0]) + hi16(a01)), pk2(lo16(hv[j][1]) + lo16(a23), hi16(hv[j][1]) + hi16(a23))};
      }
    }
  }
}
extern "C" __global__ void k9_end() {}

extern "C" void kernel_launch(void* const* d_in, const int* in_sizes, int n_in, void* d_out, int out_size, void* d_ws, size_t ws_size, hipStream_t stream) {
  const u16* q = (const u16*)d_in[0]; const u16* k = (const u16*)d_in[1]; const u16* v = (const u16*)d_in[2];
  const u16* shk = (const u16*)d_in[3]; const u16* ssmk = (const u16*)d_in[4]; const u16* ssmD = (const u16*)d_in[5];
  const u16* wg = (const u16*)d_in[7]; const u16* wu = (const u16*)d_in[8]; const u16* wd = (const u16*)d_in[9];
  u16* ws = (u16*)d_ws; const size_t SD = (size_t)S_ * D_;
  const size_t SDP = SD + (1 << 18);
  u16* kvr = ws; u16* yb = ws + SDP; u16* h = ws + 2 * SDP; u16* hn = ws + 3 * SDP; u16* Hb = ws + 4 * SDP;
  u32* xbuf = (u32*)(ws + 4 * SDP + (size_t)S_ * F_);
  unsigned long long* fl = (unsigned long long*)(xbuf + 256 * 4096);
  km_main<<<256, 256, 0, stream>>>(k, v, shk, kvr, ssmk, ssmD, yb, q, h, hn, wg, wu, wd, Hb, xbuf, fl, (u16*)d_out);
}
```
